```python
import torch

def _softmax(x, axis=-1):
    return torch.softmax(x, dim=axis)
def _gelu(x):
    return torch.nn.functional.gelu(x, approximate="tanh")
def _silu(x):
    return torch.nn.functional.silu(x)
def _sigmoid(x):
    return torch.sigmoid(x)
def _layernorm(x, axis=-1, eps=1e-5):
    m = x.mean(dim=axis, keepdim=True)
    v = x.var(dim=axis, keepdim=True, unbiased=False)
    return (x - m) / torch.sqrt(v + eps)
def _scatter_add(tgt, idx, src, axis=0):
    return tgt.clone().index_add_(axis, idx, src)
def _segment_sum(src, seg, n):
    out = torch.zeros((n,) + src.shape[1:], dtype=src.dtype)
    return out.index_add_(0, seg, src)
def _pad(x, pw):
    flat = tuple(v for pair in reversed(pw) for v in pair)
    return torch.nn.functional.pad(x, flat)

kernel_name = 'standalone__deep_cross_xdeepfm__recsys'

def setup_inputs():

    _w = (lambda a: (lambda t: t.to(torch.bfloat16) if t.is_floating_point() else t)(torch.as_tensor(a)))
    B, F, D, L = 4096, 26, 16, 6
    V = min(1000, 100000)
    Din = F * D
    return {
        "idx": _w((lambda lo, hi, *s: torch.randint(lo, hi, s))(0, V, B, F).to(torch.int64)),
        "table": _w((lambda *s: torch.randn(*s, dtype=torch.bfloat16) * 0.8)(V, D)),
        "ws_cin": _w((lambda *s: torch.randn(*s, dtype=torch.bfloat16) * 0.4)(L, F, F, F) * (1.0 / F)),
        "w_cin_out": _w((lambda *s: torch.randn(*s, dtype=torch.bfloat16) * 0.4)(L * F) * ((L * F) ** -0.5)),
        "w_dnn1": _w((lambda *s: torch.randn(*s, dtype=torch.bfloat16) * 0.8)(Din, Din) * (Din ** -0.5)),
        "b_dnn1": _w((lambda *s: torch.randn(*s, dtype=torch.bfloat16) * 3.2)(Din) * 0.1),
        "w_dnn2": _w((lambda *s: torch.randn(*s, dtype=torch.bfloat16) * 0.4)(Din) * (Din ** -0.5)),
        "b_out": _w((lambda *s: torch.randn(*s, dtype=torch.bfloat16) * 0.4)(1)),
    }

def reference(idx, table, ws_cin, w_cin_out, w_dnn1, b_dnn1, w_dnn2, b_out):
    x0 = table[idx]
    B = x0.shape[0]
    L = ws_cin.shape[0]
    xk = x0
    pooled = []
    for l in range(L):
        xk = torch.einsum('bid,bjd,hij->bhd', xk, x0, ws_cin[l])
        pooled.append(xk.sum(axis=-1))
    p_plus = torch.stack(pooled, axis=1).reshape(B, -1)
    logit_cin = p_plus @ w_cin_out
    h = x0.reshape(B, -1)
    h = _gelu(h @ w_dnn1 + b_dnn1)
    logit_dnn = h @ w_dnn2
    return logit_cin + logit_dnn + b_out
```

```cpp
#include <hip/hip_runtime.h>
#include <hip/hip_bf16.h>

static __device__ __forceinline__ float bf2f(unsigned short u) { return __uint_as_float(((unsigned)u) << 16); }

__global__ __launch_bounds__(256) void naive_kernel(const int* __restrict__ idx, const unsigned short* __restrict__ table,
    const unsigned short* __restrict__ W, const unsigned short* __restrict__ wout, const unsigned short* __restrict__ w1,
    const unsigned short* __restrict__ b1, const unsigned short* __restrict__ w2, const unsigned short* __restrict__ bout,
    __hip_bfloat16* __restrict__ out) {
  __shared__ float x0[26*16], xk[2][26*16], red[256];
  int b = blockIdx.x, t = threadIdx.x;
  for (int e = t; e < 416; e += 256) { int f = e >> 4, d = e & 15; float v = bf2f(table[idx[b*26+f]*16 + d]); x0[e] = v; xk[0][e] = v; }
  __syncthreads();
  float s = 0.f;
  for (int l = 0; l < 6; ++l) {
    const float* xi = xk[l&1]; float* xo = xk[(l+1)&1];
    for (int e = t; e < 416; e += 256) {
      int h = e >> 4, d = e & 15;
      const unsigned short* w = W + (l*26 + h) * 676;
      float a = 0.f;
      for (int i = 0; i < 26; ++i) { float u = 0.f; for (int j = 0; j < 26; ++j) u += bf2f(w[i*26+j]) * x0[j*16+d]; a += u * xi[i*16+d]; }
      xo[e] = a; s += a * bf2f(wout[l*26+h]);
    }
    __syncthreads();
  }
  for (int n = t; n < 416; n += 256) {
    float a = bf2f(b1[n]);
    for (int k = 0; k < 416; ++k) a += x0[k] * bf2f(w1[k*416+n]);
    float z = 0.7978845608028654f * (a + 0.044715f * a * a * a);
    float g = 0.5f * a * (1.f + tanhf(z));
    s += g * bf2f(w2[n]);
  }
  red[t] = s; __syncthreads();
  for (int o = 128; o > 0; o >>= 1) { if (t < o) red[t] += red[t+o]; __syncthreads(); }
  if (t == 0) out[b] = __float2bfloat16(red[0] + bf2f(bout[0]));
}

extern "C" void kernel_launch(void* const* d_in, const int* in_sizes, int n_in,
                              void* d_out, int out_size, void* d_ws, size_t ws_size,
                              hipStream_t stream) {
  naive_kernel<<<out_size, 256, 0, stream>>>((const int*)d_in[0], (const unsigned short*)d_in[1], (const unsigned short*)d_in[2],
     (const unsigned short*)d_in[3], (const unsigned short*)d_in[4], (const unsigned short*)d_in[5], (const unsigned short*)d_in[6],
     (const unsigned short*)d_in[7], (__hip_bfloat16*)d_out);
}
```

```cpp
#include <hip/hip_runtime.h>
#include <hip/hip_bf16.h>

typedef unsigned short u16;
typedef unsigned int u32;
typedef unsigned long long u64;
typedef __attribute__((ext_vector_type(2))) unsigned short u16x2;
typedef __attribute__((ext_vector_type(4))) short s16x4;
typedef __attribute__((ext_vector_type(8))) short bf16x8;
typedef __attribute__((ext_vector_type(2))) unsigned int u32x2;
typedef __attribute__((ext_vector_type(4))) unsigned int u32x4;
typedef __attribute__((ext_vector_type(4))) float f32x4;
typedef __attribute__((ext_vector_type(16))) float f32x16;
typedef __attribute__((ext_vector_type(2))) _Float16 f16x2;
#define LDS_AS __attribute__((address_space(3)))
#define GLB_AS __attribute__((address_space(1)))
#define DI __device__ __forceinline__
#define RLX_AGENT __ATOMIC_RELAXED, __HIP_MEMORY_SCOPE_AGENT
#define RLX_WG __ATOMIC_RELAXED, __HIP_MEMORY_SCOPE_WORKGROUP

constexpr int DLY0 = 24;
constexpr int DLYF = 12;
constexpr int DLY1 = 40;
constexpr int LOADER_PRIO = 0;
constexpr int NB = 4096, NV = 1000, DIN = 416;
constexpr int FRAG = 864;
constexpr int NPIECES = 148;
constexpr int NDEF = 6;
constexpr int NPREP = 31, NPQ = 16;
constexpr u32 WS_EPOCH = 0;
constexpr u32 WS_FLAG = 192 * 1024;
constexpr u32 FSTR = 128, FREP = 32 * FSTR;
constexpr u32 WS_CST = 4096;
constexpr u32 WS_ARR = 8192;
constexpr u32 WS_AF = 16384;
constexpr u32 WS_AQ = 768 * 1024;
constexpr u32 WS_PART = 256 * 1024;
constexpr u32 WS_USED = 1024 * 1024;
constexpr u32 TAG_KA = 0x9E3779B1u, TAG_KB = 0x85EBCA77u;
constexpr u32 SPIN_CAP = 1u << 18;
constexpr int L_W1I = 40960, L_TAB = 94208, L_IDX = 126976, L_REC = 143360, L_MISC = 159744;

__constant__ const u32 c_gt[2][64 * 8] __attribute__((aligned(16))) = {{0x7700038, 0x8880150, 0x9a00268, 0x3800ab8, 0x100009d8, 0x9dc0a60, 0xae809e0, 0xb7009e4, 0x4980bd0, 0x5b00ce8, 0x1000, 0x40088, 0x9e80bf8, 0x9ec0c80, 0x9f00d08, 0xd9009f4, 0x7fc00c4, 0x91401dc, 0x2f40a2c, 0x40c0b44, 0xa641000, 0xaec0a68, 0xa6c0b74, 0xbfc0a70, 0x5240c5c, 0x63c0d74, 0x1000008c, 0x900114, 0xc840a74, 0xd0c0a78, 0xd940a7c, 0xa18004c, 0x80110, 0xc0198, 0x100220, 0x1402a8, 0x9900048, 0x99400d0, 0x9980158, 0x99c01e0, 0x3300018, 0x3b8001c, 0x4400020, 0x2404c8, 0x10000af0, 0xb780af4, 0xc000af8, 0xc880afc, 0x19c0094, 0x980224, 0x2ac009c, 0xa00334, 0xd40a1c, 0xa20015c, 0xa2401e4, 0x26c0a28, 0x3bc00a4, 0x44400a8, 0xac04cc, 0x55400b0, 0x10000b7c, 0xc040b80, 0xc8c0b84, 0xd140b88, 0x5500028, 0x5d8002c, 0x300660, 0x3406e8, 0xd100b00, 0xb040d98, 0xaa00050, 0xaa400d8, 0x10000118, 0x1a0011c, 0x1200228, 0x2b00124, 0xaa80160, 0xaac01e8, 0xab00270, 0x2f80ab4, 0x5dc00b4, 0x66400b8, 0xbc06ec, 0xc00774, 0xd9c0b8c, 0xb280054, 0xb2c00dc, 0x1640b30, 0x100001a4, 0x1a8022c, 0x2b401ac, 0x33c01b0, 0x1ec0b34, 0x2740b38, 0x2fc0b3c, 0xb400384, 0x1280338, 0x12c03c0, 0x4480130, 0x13404d0, 0xc081000, 0xc0c0c90, 0xc100d18, 0xc140da0, 0x1380558, 0x13c05e0, 0x6680140, 0x14406f0, 0xbb00058, 0xe00bb4, 0xbb80168, 0x1f00bbc, 0x3c401b4, 0x1b8044c, 0x4d401bc, 0x55c01c0, 0xc941000, 0xc980d1c, 0xc9c0da4, 0x5c0c38, 0x5e401c4, 0x1c8066c, 0x1cc06f4, 0x1d0077c, 0xc3c00e4, 0xc40016c, 0x1f40c44, 0xc48027c, 0x1480778, 0x14c0800, 0x2301000, 0x2b80234, 0x2780bc0, 0x3000bc4, 0x3880bc8, 0xbcc0410, 0x2380340, 0x3c8023c, 0x4500240, 0x24404d8, 0x10000d20, 0xd240da8, 0x600cc0, 0xcc400e8, 0x80401d4, 0x1d8088c, 0x100002bc, 0x2c00344, 0xc4c0304, 0xc50038c, 0xc540414, 0xc58049c, 0x2c403cc, 0x45402c8, 0x2cc04dc, 0x56402d0, 0x10000dac, 0x640d48, 0xd4c00ec, 0x1740d50, 0x5600248, 0x24c05e8, 0x6700250, 0x6f80254, 0xcc80170, 0xccc01f8, 0xcd00280, 0x3080cd4, 0x2580780, 0x25c0808, 0x2600890, 0x9180264, 0xcd80390, 0xcdc0418, 0xce004a0, 0x5280ce4, 0x2d405ec, 0x67402d8, 0x2dc06fc, 0x2e00784, 0x1fc0d54, 0xd580284, 0xd5c030c, 0xd600394, 0x2e4080c, 0x2e80894, 0x91c02ec, 0x2f009a4, 0xd64041c, 0xd6804a4, 0xd6c052c, 0x5b40d70, 0x10000348, 0x3d0034c, 0x3500458, 0x35404e0, 0x681000, 0x10000178, 0x10000288, 0x10000398, 0x3580568, 0x5f0035c, 0x6780360, 0x3640700, 0x100004a8, 0x5b81000, 0x6c81000, 0x100007d8, 0x100003d4, 0x45c03d8, 0x4e403dc, 0x3e0056c, 0xf01000, 0x2001000, 0x10000310, 0x4201000, 0x5f403e4, 0x3e8067c, 0x3ec0704, 0x3f0078c, 0x5301000, 0x10000640, 0x10000750, 0x8601000, 0x3680788, 0x36c0810, 0x8980370, 0x3740920, 0x100008e8, 0x9f81000, 0xb081000, 0xc181000, 0x37809a8, 0x37c0a30, 0x4601000, 0x4e80464, 0x10000d28, 0x10001000, 0x10001000, 0x10001000, 0x81403f4, 0x89c03f8, 0x3fc0924, 0x9ac0400, 0x9701000, 0x10000a80, 0x10000b90, 0x10000ca0, 0xa340404, 0x4080abc, 0x4ec1000, 0x57404f0, 0x10000db0, 0x10001000, 0x10001000, 0x10001000, 0x5700468, 0x5f8046c, 0x4700680, 0x7080474, 0x10001000, 0x10001000, 0x10001000, 0x10001000, 0x4780790, 0x818047c, 0x8a00480, 0x9280484, 0x10001000, 0x10001000, 0x10001000, 0x10001000, 0x4f405fc, 0x68404f8, 0x70c04fc, 0x5000794, 0x10001000, 0x10001000, 0x10001000, 0x10001000, 0x504081c, 0x8a40508, 0x50c092c, 0x9b40510, 0x10001000, 0x10001000, 0x10001000, 0x10001000, 0x9b00488, 0x48c0a38, 0xac00490, 0xb480494, 0x10001000, 0x10001000, 0x10001000, 0x10001000, 0x10000578, 0x57c0600, 0x5800688, 0x5840710, 0x10001000, 0x10001000, 0x10001000, 0x10001000, 0x5140a3c, 0x5180ac4, 0xb4c051c, 0xbd40520, 0x10001000, 0x10001000, 0x10001000, 0x10001000, 0x10000604, 0x68c0608, 0x714060c, 0x610079c, 0x10001000, 0x10001000, 0x10001000, 0x10001000, 0x5880798, 0x820058c, 0x59008a8, 0x5940930, 0x10001000, 0x10001000, 0x10001000, 0x10001000, 0x9b80598, 0x59c0a40, 0x5a00ac8, 0xb5005a4, 0x10001000, 0x10001000, 0x10001000, 0x10001000, 0x8240614, 0x8ac0618, 0x61c0934, 0x62009bc, 0x10001000, 0x10001000, 0x10001000, 0x10001000, 0xa440624, 0xacc0628, 0xb54062c, 0xbdc0630, 0x10001000, 0x10001000, 0x10001000, 0x10001000, 0x5a80bd8, 0xc6005ac, 0x6901000, 0x6940718, 0x10001000, 0x10001000, 0x10001000, 0x10001000, 0x69807a0, 0x828069c, 0x8b006a0, 0x93806a4, 0x10001000, 0x10001000, 0x10001000, 0x10001000, 0x6340c64, 0xcec0638, 0x71c1000, 0x7a40720, 0x10001000, 0x10001000, 0x10001000, 0x10001000, 0x724082c, 0x72808b4, 0x72c093c, 0x73009c4, 0x10001000, 0x10001000, 0x10001000, 0x10001000, 0x9c006a8, 0x6ac0a48, 0xad006b0, 0xb5806b4, 0x10001000, 0x10001000, 0x10001000, 0x10001000, 0xbe006b8, 0x6bc0c68, 0x6c00cf0, 0x6c40d78, 0x10001000, 0x10001000, 0x10001000, 0x10001000, 0xa4c0734, 0x7380ad4, 0xb5c073c, 0x7400be4, 0x10001000, 0x10001000, 0x10001000, 0x10001000, 0xc6c0744, 0x7480cf4, 0xd7c074c, 0x10001000, 0x10001000, 0x10001000, 0x10001000, 0x10001000, 0x100007a8, 0x83007ac, 0x7b008b8, 0x94007b4, 0x10001000, 0x10001000, 0x10001000, 0x10001000, 0x9c807b8, 0xa5007bc, 0xad807c0, 0xb6007c4, 0x10001000, 0x10001000, 0x10001000, 0x10001000, 0x10000834, 0x83808bc, 0x83c0944, 0x9cc0840, 0x10001000, 0x10001000, 0x10001000, 0x10001000, 0x8440a54, 0x8480adc, 0xb64084c, 0xbec0850, 0x10001000, 0x10001000, 0x10001000, 0x10001000, 0xbe807c8, 0xc7007cc, 0x7d00cf8, 0x7d40d80, 0x10001000, 0x10001000, 0x10001000, 0x10001000, 0x100008c0, 0x8c40948, 0x8c809d0, 0xa5808cc, 0x10001000, 0x10001000, 0x10001000, 0x10001000, 0x8540c74, 0x8580cfc, 0xd84085c, 0x3c07f8, 0x10001000, 0x10001000, 0x10001000, 0x10001000, 0x1000094c, 0x9d40950, 0xa5c0954, 0xae40958, 0x10001000, 0x10001000, 0x10001000, 0x10001000, 0x8d00ae0, 0xb6808d4, 0xbf008d8, 0xc7808dc, 0x10001000, 0x10001000, 0x10001000, 0x10001000, 0x8e00d00, 0xd8808e4, 0x8800040, 0x88400c8, 0x10001000, 0x10001000, 0x10001000, 0x10001000, 0x95c0b6c, 0xbf40960, 0xc7c0964, 0x9680d04, 0x10001000, 0x10001000, 0x10001000, 0x10001000, 0xd8c096c, 0x9080044, 0xcc090c, 0x9100154, 0x10001000, 0x10001000, 0x10001000, 0x10001000}, {0x1c02d8, 0x880344, 0xf403b0, 0x160041c, 0xf00003cc, 0x40003ce, 0x43403d0, 0x46803d2, 0x1cc0488, 0x23804f4, 0xf0000000, 0x340002, 0x49c03d4, 0x4d003d6, 0x50403d8, 0x53803da, 0x52030e, 0xbe037a, 0x12a03e6, 0x1960452, 0xf0000402, 0x4360404, 0x46a0406, 0x49e0408, 0x20204be, 0x26e052a, 0xf0000036, 0x6a0038, 0x4d2040a, 0x506040c, 0x53a040e, 0x2603dc, 0x680004, 0x9c0006, 0xd00008, 0x104000a, 0x2403a8, 0x5803aa, 0x8c03ac, 0xc003ae, 0x138000c, 0x16c000e, 0x1a00010, 0x1d40012, 0xf0000438, 0x46c043a, 0x4a0043c, 0x4d4043e, 0x9e003a, 0xd2003c, 0x106003e, 0x13a0040, 0x5a03de, 0x8e03e0, 0xc203e2, 0xf603e4, 0x16e0042, 0x1a20044, 0x1d60046, 0x20a0048, 0xf000046e, 0x4a20470, 0x4d60472, 0x50a0474, 0x2080014, 0x23c0016, 0x2700018, 0x2a4001a, 0x5080440, 0x53c0442, 0x280410, 0x5c0412, 0xf000006c, 0xa0006e, 0xd40070, 0x1080072, 0x900414, 0xc40416, 0xf80418, 0x12c041a, 0x23e004a, 0x272004c, 0x2a6004e, 0x2da0050, 0x53e0476, 0x2a0444, 0x5e0446, 0x920448, 0xf00000a2, 0xd600a4, 0x10a00a6, 0x13e00a8, 0xc6044a, 0xfa044c, 0x12e044e, 0x1620450, 0x13c0074, 0x1700076, 0x1a40078, 0x1d8007a, 0xf00004a4, 0x4d804a6, 0x50c04a8, 0x54004aa, 0x20c007c, 0x240007e, 0x2740080, 0x2a80082, 0x2c0478, 0x60047a, 0x94047c, 0xc8047e, 0x17200aa, 0x1a600ac, 0x1da00ae, 0x20e00b0, 0xf00004da, 0x50e04dc, 0x54204de, 0x2e04ac, 0x24200b2, 0x27600b4, 0x2aa00b6, 0x2de00b8, 0x6204ae, 0x9604b0, 0xca04b2, 0xfe04b4, 0x2dc0084, 0x3100086, 0xf00000d8, 0x10c00da, 0xfc0480, 0x1300482, 0x1640484, 0x1980486, 0x14000dc, 0x17400de, 0x1a800e0, 0x1dc00e2, 0xf0000510, 0x5440512, 0x3004e0, 0x6404e2, 0x31200ba, 0x34600bc, 0xf000010e, 0x1420110, 0x13204b6, 0x16604b8, 0x19a04ba, 0x1ce04bc, 0x1760112, 0x1aa0114, 0x1de0116, 0x2120118, 0xf0000546, 0x320514, 0x660516, 0x9a0518, 0x21000e4, 0x24400e6, 0x27800e8, 0x2ac00ea, 0x9804e4, 0xcc04e6, 0x10004e8, 0x13404ea, 0x2e000ec, 0x31400ee, 0x34800f0, 0x37c00f2, 0x16804ec, 0x19c04ee, 0x1d004f0, 0x20404f2, 0x246011a, 0x27a011c, 0x2ae011e, 0x2e20120, 0xce051a, 0x102051c, 0x136051e, 0x16a0520, 0x3160122, 0x34a0124, 0x37e0126, 0x3b20128, 0x19e0522, 0x1d20524, 0x2060526, 0x23a0528, 0xf0000144, 0x1780146, 0x1ac0148, 0x1e0014a, 0xf000f000, 0xf000f000, 0xf000f000, 0xf000f000, 0x214014c, 0x248014e, 0x27c0150, 0x2b00152, 0xf000f000, 0xf000f000, 0xf000f000, 0xf000f000, 0xf000017a, 0x1ae017c, 0x1e2017e, 0x2160180, 0xf000f000, 0xf000f000, 0xf000f000, 0xf000f000, 0x24a0182, 0x27e0184, 0x2b20186, 0x2e60188, 0xf000f000, 0xf000f000, 0xf000f000, 0xf000f000, 0x2e40154, 0x3180156, 0x34c0158, 0x380015a, 0xf000f000, 0xf000f000, 0xf000f000, 0xf000f000, 0x3b4015c, 0x3e8015e, 0xf00001b0, 0x1e401b2, 0xf000f000, 0xf000f000, 0xf000f000, 0xf000f000, 0x31a018a, 0x34e018c, 0x382018e, 0x3b60190, 0xf000f000, 0xf000f000, 0xf000f000, 0xf000f000, 0x3ea0192, 0x41e0194, 0xf00001e6, 0x21a01e8, 0xf000f000, 0xf000f000, 0xf000f000, 0xf000f000, 0x21801b4, 0x24c01b6, 0x28001b8, 0x2b401ba, 0xf000f000, 0xf000f000, 0xf000f000, 0xf000f000, 0x2e801bc, 0x31c01be, 0x35001c0, 0x38401c2, 0xf000f000, 0xf000f000, 0xf000f000, 0xf000f000, 0x24e01ea, 0x28201ec, 0x2b601ee, 0x2ea01f0, 0xf000f000, 0xf000f000, 0xf000f000, 0xf000f000, 0x31e01f2, 0x35201f4, 0x38601f6, 0x3ba01f8, 0xf000f000, 0xf000f000, 0xf000f000, 0xf000f000, 0x3b801c4, 0x3ec01c6, 0x42001c8, 0x45401ca, 0xf000f000, 0xf000f000, 0xf000f000, 0xf000f000, 0xf000021c, 0x250021e, 0x2840220, 0x2b80222, 0xf000f000, 0xf000f000, 0xf000f000, 0xf000f000, 0x3ee01fa, 0x42201fc, 0x45601fe, 0x48a0200, 0xf000f000, 0xf000f000, 0xf000f000, 0xf000f000, 0xf0000252, 0x2860254, 0x2ba0256, 0x2ee0258, 0xf000f000, 0xf000f000, 0xf000f000, 0xf000f000, 0x2ec0224, 0x3200226, 0x3540228, 0x388022a, 0xf000f000, 0xf000f000, 0xf000f000, 0xf000f000, 0x3bc022c, 0x3f0022e, 0x4240230, 0x4580232, 0xf000f000, 0xf000f000, 0xf000f000, 0xf000f000, 0x322025a, 0x356025c, 0x38a025e, 0x3be0260, 0xf000f000, 0xf000f000, 0xf000f000, 0xf000f000, 0x3f20262, 0x4260264, 0x45a0266, 0x48e0268, 0xf000f000, 0xf000f000, 0xf000f000, 0xf000f000, 0x48c0234, 0x4c00236, 0xf0000288, 0x2bc028a, 0xf000f000, 0xf000f000, 0xf000f000, 0xf000f000, 0x2f0028c, 0x324028e, 0x3580290, 0x38c0292, 0xf000f000, 0xf000f000, 0xf000f000, 0xf000f000, 0x4c2026a, 0x4f6026c, 0xf00002be, 0x2f202c0, 0xf000f000, 0xf000f000, 0xf000f000, 0xf000f000, 0x32602c2, 0x35a02c4, 0x38e02c6, 0x3c202c8, 0xf000f000, 0xf000f000, 0xf000f000, 0xf000f000, 0x3c00294, 0x3f40296, 0x4280298, 0x45c029a, 0xf000f000, 0xf000f000, 0xf000f000, 0xf000f000, 0x490029c, 0x4c4029e, 0x4f802a0, 0x52c02a2, 0xf000f000, 0xf000f000, 0xf000f000, 0xf000f000, 0x3f602ca, 0x42a02cc, 0x45e02ce, 0x49202d0, 0xf000f000, 0xf000f000, 0xf000f000, 0xf000f000, 0x4c602d2, 0x4fa02d4, 0x52e02d6, 0xf000f000, 0xf000f000, 0xf000f000, 0xf000f000, 0xf000f000, 0xf00002f4, 0x32802f6, 0x35c02f8, 0x39002fa, 0xf000f000, 0xf000f000, 0xf000f000, 0xf000f000, 0x3c402fc, 0x3f802fe, 0x42c0300, 0x4600302, 0xf000f000, 0xf000f000, 0xf000f000, 0xf000f000, 0xf000032a, 0x35e032c, 0x392032e, 0x3c60330, 0xf000f000, 0xf000f000, 0xf000f000, 0xf000f000, 0x3fa0332, 0x42e0334, 0x4620336, 0x4960338, 0xf000f000, 0xf000f000, 0xf000f000, 0xf000f000, 0x4940304, 0x4c80306, 0x4fc0308, 0x530030a, 0xf000f000, 0xf000f000, 0xf000f000, 0xf000f000, 0xf0000360, 0x3940362, 0x3c80364, 0x3fc0366, 0xf000f000, 0xf000f000, 0xf000f000, 0xf000f000, 0x4ca033a, 0x4fe033c, 0x532033e, 0x1e030c, 0xf000f000, 0xf000f000, 0xf000f000, 0xf000f000, 0xf0000396, 0x3ca0398, 0x3fe039a, 0x432039c, 0xf000f000, 0xf000f000, 0xf000f000, 0xf000f000, 0x4300368, 0x464036a, 0x498036c, 0x4cc036e, 0xf000f000, 0xf000f000, 0xf000f000, 0xf000f000, 0x5000370, 0x5340372, 0x200340, 0x540342, 0xf000f000, 0xf000f000, 0xf000f000, 0xf000f000, 0x466039e, 0x49a03a0, 0x4ce03a2, 0x50203a4, 0xf000f000, 0xf000f000, 0xf000f000, 0xf000f000, 0x53603a6, 0x220374, 0x560376, 0x8a0378, 0xf000f000, 0xf000f000, 0xf000f000, 0xf000f000}};

DI float bf2f(u32 u) { return __uint_as_float(u << 16); }
DI float bfhi(u32 u) { return __uint_as_float(u & 0xffff0000u); }
DI u16 f2bf(float f) { return __builtin_bit_cast(u16, __float2bfloat16(f)); }
DI u32 pack_h2(float a, float b) { f16x2 v = {(_Float16)a, (_Float16)b}; return __builtin_bit_cast(u32, v); }

DI int row_slot(int o) {
  const int hp = o >= 13, reg = o - 13 * hp;
  const int base = (int)((0x1A0C081604120E00ull >> (8 * (2 * (reg >> 2) + hp))) & 0xff);
  return o == 26 ? 13 : base + (reg & 3);
}

struct Args {
  const int* idx; const u16* table; const u16* W; const u16* wout; const u16* w1; const u16* b1; const u16* w2; const u16* bout;
  u16* out; char* ws;
};

DI u32 pkmax(u32 a, u32 b) { return __builtin_bit_cast(u32, __builtin_elementwise_max(__builtin_bit_cast(u16x2, a), __builtin_bit_cast(u16x2, b))); }
DI u32 wave_max2(u32 v) {
  v = pkmax(v, __builtin_amdgcn_update_dpp(0u, v, 0x128, 0xf, 0xf, false));
  v = pkmax(v, __builtin_amdgcn_update_dpp(0u, v, 0x124, 0xf, 0xf, false));
  v = pkmax(v, __builtin_amdgcn_update_dpp(0u, v, 0x122, 0xf, 0xf, false));
  v = pkmax(v, __builtin_amdgcn_update_dpp(0u, v, 0x121, 0xf, 0xf, false));
  auto r = __builtin_amdgcn_permlane16_swap(v, v, false, false); v = pkmax(r[0], r[1]);
  auto s = __builtin_amdgcn_permlane32_swap(v, v, false, false); v = pkmax(s[0], s[1]);
  return v;
}
DI int expo16(u32 m) { return (int)(m >> 7) - 126; }
DI u64 make_tag(u64 e) { return (u64)((u32)e + TAG_KA) | ((u64)((u32)(e >> 32) + TAG_KB) << 32); }
DI void store16_dev(__amdgpu_buffer_rsrc_t rs, u32 off, u32x4 v) { __builtin_amdgcn_raw_buffer_store_b128(v, rs, off, 0, 16); }

constexpr int GP = 40;
#define PIN(x) asm volatile("" : "+v"(x))
#define BAR_VM(n) asm volatile("s_waitcnt vmcnt(" #n ") lgkmcnt(0)\n\ts_barrier" ::: "memory")
#define BAR_LG() asm volatile("s_waitcnt lgkmcnt(0)\n\ts_barrier" ::: "memory")
DI void dma16(const char* base, u32 off, u32 ldst) {
  asm volatile("s_mov_b32 m0, %2\n\ts_nop 0\n\tglobal_load_lds_dwordx4 %0, %1" :: "v"(off), "s"(base), "s"(ldst) : "memory");
}
DI u32 lds32(u32 p) { return *(const LDS_AS u32*)(size_t)p; }
DI u16 lds16(u32 p) { return *(const LDS_AS u16*)(size_t)p; }
DI u32x4 lds128(u32 p) { return *(const LDS_AS u32x4*)(size_t)p; }
DI u32 absmax8(u32x4 t) { return pkmax(pkmax(t[0] & 0x7fff7fffu, t[1] & 0x7fff7fffu), pkmax(t[2] & 0x7fff7fffu, t[3] & 0x7fff7fffu)); }

DI void dma4(const char* base, u32 off, u32 ldst) {
  asm volatile("s_mov_b32 m0, %2\n\ts_nop 0\n\tglobal_load_lds_dword %0, %1" :: "v"(off), "s"(base), "s"(ldst) : "memory");
}
typedef __attribute__((ext_vector_type(8))) unsigned short u16x8;
DI s16x4 ldstr(u32 p) { return __builtin_amdgcn_ds_read_tr16_b64_v4i16((LDS_AS s16x4*)(size_t)p); }
DI bf16x8 cat8(s16x4 x, s16x4 y) { return __builtin_shufflevector(x, y, 0, 1, 2, 3, 4, 5, 6, 7); }

DI void wait_lds(LDS_AS u32* w, u32 want) {
  _Pragma("nounroll") for (u32 spins = 0; spins < 64 * SPIN_CAP; ++spins) {
    if (__hip_atomic_load(w, RLX_WG) >= want) break;
    __builtin_amdgcn_s_sleep(1);
  }
  asm volatile("" ::: "memory");
}

DI void publish(const Args& a, u32 cnt, u32 nw, u64 tag, int pid, int lane) {
  asm volatile("s_waitcnt vmcnt(0)" ::: "memory");
  u32 old = 0;
  if (lane == 0) old = __hip_atomic_fetch_add((LDS_AS u32*)(size_t)cnt, 1u, RLX_WG);
  if (__builtin_amdgcn_readfirstlane(old) == nw - 1 && lane < 8) __hip_atomic_store((GLB_AS u64*)(a.ws + WS_FLAG + lane * FREP + pid * FSTR), tag, RLX_AGENT);
}

template <int ROLE, class F>
DI void prepq(const Args& a, u32 lb, u32 own, int sub, int tid, int lane, int wave, F front) {
  constexpr u32 L_G = 35840, L_RED = L_G + 5120, L_R = L_RED + 256;
  const char* W = (const char*)a.W;
  const int r = lane & 31, hh = lane >> 5, i16 = lane & 15, g4 = lane >> 4, rc = min(r, 25);
  auto scan = [&]() {
    u32 mw = 0;
#pragma unroll
    for (int i = 0; i < 5; ++i) mw = pkmax(mw, absmax8(lds128(own + i * 1024)));
    *(LDS_AS u32*)(size_t)(lb + L_RED + wave * 4) = wave_max2(max(mw & 0xffffu, mw >> 16));
  };
  if (wave < 4) {
    const int reg = (sub & 3) * 4 + wave;
    const int o = ROLE == 2 ? 26 + wave : ROLE == 1 ? sub * 4 + wave : reg < 13 ? (sub >> 2) * 13 + reg : 13 + reg + (sub >> 2);
    const int oc = min(o, ROLE == 0 ? (sub >> 2) * 13 + 12 : 25);
    u32x4 gt0, gt1; u64 ep;
    const u32 rb = lb + L_R + wave * 2304;
    if (ROLE == 0) {
#pragma unroll
      for (int p = 0; p < 7; ++p) dma4(W, 4 * 35152 + min(4 * p + g4, 25) * 1352 + oc * 52 + min(i16, 12) * 4, rb + p * 256);
    }
    asm volatile("global_load_dwordx2 %0, %3, %4 sc1\n\tglobal_load_dwordx4 %1, %5, %6\n\tglobal_load_dwordx4 %2, %5, %6 offset:16"
                 : "=&v"(ep), "=&v"(gt0), "=&v"(gt1) : "v"(0u), "s"(a.ws), "v"(lane * 32), "s"(&c_gt[ROLE ? 1 : 0][0]) : "memory");
    if (ROLE == 1) { asm volatile("s_waitcnt vmcnt(3)" ::: "memory"); scan(); }
    const u32 mb = ROLE == 0 ? lb + wave * 4352 : ROLE == 1 ? lb + oc * 1352 : lb + L_G;
    *(LDS_AS u32x4*)(size_t)(lb + 35152 + min(lane, 42) * 16) = u32x4{0u, 0u, 0u, 0u};
    if (ROLE == 0) { *(LDS_AS u32x4*)(size_t)(lb + L_G + tid * 16) = u32x4{0u, 0u, 0u, 0u}; *(LDS_AS u32x4*)(size_t)(lb + L_G + 4096 + lane * 16) = u32x4{0u, 0u, 0u, 0u}; }
    const u32 fb = rb + 32 * (g4 & 1) + 8 * (i16 & 3);
    const int h0 = 8 * hh + (i16 >> 2);
    u32 fa0 = fb + h0 * 64, fa2 = fb + min(h0 + 16, 25) * 64, fa3 = fb + min(h0 + 20, 25) * 64;
    u32 gf = lb + L_G + (r * GP + 8 * hh) * 2, dq = r < 27 ? mb + (r + 4 * hh * 34) * 4 : 0x100000u;
    u32 gd = o < (ROLE == 1 ? 26 : 27) ? WS_AQ + (ROLE ? 65536 : 0) + row_slot(min(o, 26)) * 2048 + lane * 16 : 0x7f000000u;
    u32 gd2 = lane < 32 ? gd + 1024 : lane < 40 && wave == 0 && (ROLE != 1 || sub == 0) ? WS_AQ + 27 * 2048 + (ROLE ? 7 + ROLE : sub) * 128 + (lane - 32) * 16 : 0x7f000000u;
    PIN(fa0); PIN(fa2); PIN(fa3); PIN(gf); PIN(dq); PIN(gd); PIN(gd2);
    if (ROLE == 0) BAR_VM(10); else if (ROLE == 2) BAR_VM(3);
    asm volatile("s_waitcnt vmcnt(0)" : "+v"(ep), "+v"(gt0), "+v"(gt1) :: "memory");
    bf16x8 f0, f1;
    if (ROLE == 0) { f0 = cat8(ldstr(fa0), ldstr(fa0 + 256)); f1 = cat8(ldstr(fa2), ldstr(fa3)); __builtin_amdgcn_sched_barrier(0); }
    ep = make_tag(ep);
    PIN(ep);
    u32 ga[16];
#pragma unroll
    for (int e = 0; e < 8; ++e) {
      const u32 t = e < 4 ? gt0[e] : gt1[e - 4];
      if (ROLE == 0) { ga[2 * e] = mb + (t & 0xffff); ga[2 * e + 1] = mb + (t >> 16); }
      else if (ROLE == 1) { ga[2 * e] = min(mb + (t & 0xffff), lb + 35152); ga[2 * e + 1] = min(mb + (t >> 16), lb + 35152); }
      else { ga[2 * e] = (mb + ((t & 0xffff) << 1)) | ((t << 5) & 0x100000u); ga[2 * e + 1] = (mb + ((t >> 16) << 1)) | ((t >> 11) & 0x100000u); }
      PIN(ga[2 * e]); PIN(ga[2 * e + 1]);
    }
    u32 fa[16];
    u32x4 t0, t1;
    if (ROLE == 1) {
      BAR_LG();
#pragma unroll
      for (int e = 0; e < 16; ++e) fa[e] = (u32)lds16(ga[e]) << 16;
      t0 = lds128(lb + L_RED); t1 = lds128(lb + L_RED + 16);
    }
    if (ROLE == 0) {
      const u32 mw = pkmax(absmax8(__builtin_bit_cast(u32x4, f0)), absmax8(__builtin_bit_cast(u32x4, f1)));
      *(LDS_AS u32*)(size_t)(lb + L_RED + wave * 4) = wave_max2(max(mw & 0xffffu, mw >> 16));
      __builtin_amdgcn_s_setprio(3);
      BAR_LG();
      u32x4 g[4];
#pragma unroll
      for (int i = 0; i < 4; ++i) g[i] = lds128(gf + (i >> 1) * 32 * GP * 2 + (i & 1) * 32);
      t0 = lds128(lb + L_RED);
      *(LDS_AS u32*)(size_t)(mb + 4096) = 0u;
      __builtin_amdgcn_sched_barrier(0);
      f32x16 q;
#pragma unroll
      for (int i = 0; i < 16; ++i) q[i] = 0.f;
#pragma unroll
      for (int i = 0; i < 4; ++i) q = __builtin_amdgcn_mfma_f32_32x32x16_bf16((i & 1) ? f1 : f0, __builtin_bit_cast(bf16x8, g[i]), q, 0, 0, 0);
      u32 mg = pkmax(absmax8(g[0]), absmax8(g[1]));
      mg = max(mg & 0xffffu, mg >> 16) << (r == 26 ? 16 : 0);
      PIN(mg);
      __builtin_amdgcn_sched_barrier(0);
#pragma unroll
      for (int i = 0; i < 14; ++i) *(LDS_AS u32*)(size_t)(dq + ((i & 3) + 8 * (i >> 2)) * 136) = __float_as_uint(q[i]);
#pragma unroll
      for (int e = 0; e < 16; ++e) fa[e] = lds32(ga[e]);
      t1[0] = wave_max2(mg);
    }
    if (ROLE == 2) {
      BAR_LG();
#pragma unroll
      for (int e = 0; e < 16; ++e) fa[e] = lds32(ga[e]);
      t0 = lds128(lb + L_RED);
    }
    u32 mm = pkmax(pkmax(t0[0], t0[1]), pkmax(t0[2], t0[3]));
    if (ROLE == 1) mm = pkmax(mm, pkmax(pkmax(t1[0], t1[1]), pkmax(t1[2], t1[3])));
    const int a0 = ROLE ? expo16(mm & 0xffffu) - 14 : min(expo16(mm) + expo16(t1[0] & 0xffffu) - 9, 114);
    const int a1 = ROLE ? expo16(mm >> 16) - 14 : min(expo16(mm) + expo16(t1[0] >> 16) - 10, 114);
    int s1 = ROLE == 0 ? (o == 26 ? -1000 : -a0) : ROLE == 1 ? -a0 : -a1;
    int s2 = ROLE == 0 && o != 26 && lane >= 24 ? -a1 : s1;
    if (ROLE == 0) {
      __builtin_amdgcn_sched_group_barrier(0x002, 6, 0);
#pragma unroll
      for (int i = 0; i < 23; ++i) { __builtin_amdgcn_sched_group_barrier(0x080, 1, 0); __builtin_amdgcn_sched_group_barrier(0x002, 2, 0); }
      PIN(s1); PIN(s2); __builtin_amdgcn_sched_barrier(0);
    }
    float gx[8];
#pragma unroll
    for (int e = 0; e < 8; ++e) { gx[e] = __uint_as_float(fa[2 * e]) + __uint_as_float(fa[2 * e + 1]); }
    u32x4 z;
#pragma unroll
    for (int e = 0; e < 4; ++e) z[e] = pack_h2(ldexpf(gx[2 * e], e < 2 ? s1 : s2), ldexpf(gx[2 * e + 1], e < 2 ? s1 : s2));
    if (lane >= 32) { z[2] = a0; z[3] = a1; }
    const __amdgpu_buffer_rsrc_t rs = __builtin_amdgcn_make_buffer_rsrc(a.ws, 0, WS_USED, 0x00020000);
    const u32 ta = (u32)ep, tb = (u32)(ep >> 32);
    store16_dev(rs, gd, u32x4{z[0], z[1], ta ^ z[0], tb ^ z[1]});
    store16_dev(rs, gd2, u32x4{z[2], z[3], ta ^ z[2], tb ^ z[3]});
    __builtin_amdgcn_s_sleep(ROLE ? DLY1 : DLY0);
  } else {
    const int hw = wave - 4;
    if (ROLE == 1) {
      asm volatile("s_waitcnt vmcnt(0)" ::: "memory");
      scan();
      BAR_LG();
      __builtin_amdgcn_s_sleep(DLYF);
      front();
    } else {
      u32 mv = 0;
      u32x4 wf; u32 w4;
      asm volatile("global_load_dwordx4 %0, %2, %4\n\tglobal_load_ushort %1, %3, %4" : "=&v"(wf), "=&v"(w4)
                   : "v"((ROLE == 0 ? 260 : 0) + (g4 < 3 ? 16 * g4 : 36)), "v"(208 + 2 * rc), "s"(a.wout) : "memory");
      if (ROLE == 0) front();
      const u32 tb = lb + (hw * 192 + 4 * (i16 & 3)) * 2;
      const int h0 = 8 * g4 + (i16 >> 2);
      u32 ta0 = tb + min(h0, 25) * 1352, ta1 = tb + min(h0 + 4, 25) * 1352;
      u32 go[3], gm[3];
#pragma unroll
      for (int i = 0; i < 3; ++i) {
        const u32 c = hw * 192 + i * 64 + lane, m = c / 26;
        go[i] = c < 676 ? lb + L_G + (ROLE == 0 ? ((c - 26 * m) * GP + m) * 2 : c * 4) : 0x100000u;
        gm[i] = c < 676 ? 0x7fffffffu : 0u;
        PIN(go[i]); PIN(gm[i]);
      }
      PIN(ta0); PIN(ta1);
      __builtin_amdgcn_s_setprio(3);
      u32 qm[4];
#pragma unroll
      for (int q = 0; q < 4; ++q) { qm[q] = (i16 >> 2) == q ? ~0u : 0u; PIN(qm[q]); }
      if (ROLE == 0) BAR_VM(12); else BAR_VM(2);
      bf16x8 tr[12];
#pragma unroll
      for (int i = 0; i < 12; ++i) tr[i] = cat8(ldstr(ta0 + i * 32), ldstr(ta1 + i * 32));
      __builtin_amdgcn_sched_barrier(0);
      asm volatile("s_waitcnt vmcnt(%2)" : "+v"(wf), "+v"(w4) : "i"(ROLE == 0 ? 10 : 0));
      if (g4 == 3) wf = u32x4{wf[3], 0u, 0u, 0u};
      u32x4 wq[4];
#pragma unroll
      for (int q = 0; q < 4; ++q) wq[q] = wf & qm[q];
      f32x4 d[3];
#pragma unroll
      for (int i = 0; i < 3; ++i) {
        d[i] = f32x4{0.f, 0.f, 0.f, 0.f};
#pragma unroll
        for (int q = 0; q < 4; ++q) d[i] = __builtin_amdgcn_mfma_f32_16x16x32_bf16(__builtin_bit_cast(bf16x8, wq[q]), tr[4 * i + q], d[i], 0, 0, 0);
      }
#pragma unroll
      for (int i = 0; i < 3; ++i) {
        const float x = d[i][0];
        if (ROLE == 0) { const u16 gh = f2bf(x); *(LDS_AS u16*)(size_t)go[i] = gh; *(LDS_AS u16*)(size_t)(go[i] + 32 * GP * 2) = f2bf(x - bf2f(gh)); }
        else { mv = max(mv, __float_as_uint(x) & gm[i]); *(LDS_AS float*)(size_t)go[i] = x; }
      }
      if (ROLE == 0) *(LDS_AS u16*)(size_t)(lb + L_G + (26 * GP + rc) * 2) = (u16)w4;
      else *(LDS_AS u32*)(size_t)(lb + L_RED + hw * 4) = wave_max2(mv & 0xffff0000u);
      BAR_LG();
      __builtin_amdgcn_s_setprio(0);
      if (ROLE == 2) { __builtin_amdgcn_s_sleep(DLYF / 2); front(); }
    }
  }
}

template <int ROLE, class F>
DI void prep(const Args& a, u32 lb, u32 own, int pid, int layer, int sub, int tid, int lane, int wave, F front) {
  constexpr u32 L_V = 40960, L_RED = L_V + 5120;
  const int r = lane & 31, hh = lane >> 5, i16 = lane & 15;
  u32x4 wf0, wf1; u64 ep;
  *(LDS_AS u32*)(size_t)(lb + L_RED + 32) = 0u;
  asm volatile("global_load_dwordx2 %0, %3, %4 sc1\n\tglobal_load_dwordx4 %1, %5, %7\n\tglobal_load_dwordx4 %2, %6, %7"
               : "=&v"(ep), "=&v"(wf0), "=&v"(wf1)
               : "v"(0u), "s"(a.ws), "v"(layer * 52 + 16 * hh), "v"(layer * 52 + 32 + 4 * hh), "s"(a.wout) : "memory");
  const u32 tb = lb + (wave * 96 + 16 * ((lane >> 4) & 1) + 4 * (i16 & 3)) * 2;
  const int h0 = 8 * hh + (i16 >> 2);
  const u32 ta0 = tb + h0 * 1352, ta2 = tb + min(h0 + 16, 25) * 1352, ta3 = tb + min(h0 + 20, 25) * 1352;
  u32 ca[2][4], dst[2]; bool vr[2];
  if (wave < 4) {
#pragma unroll
    for (int i = 0; i < 2; ++i) {
      const int ch = sub * 465 + min(tid + 256 * i, sub == 4 ? 461 : 464);
      const int kh = ch / 27, row = ch - 27 * kh, k = kh >> 1, h2 = kh & 1;
      dst[i] = WS_AF + (ROLE == 3 ? 89 : layer == 1 ? 46 : 132) * FRAG + kh * 432 + row_slot(row) * 16;
      vr[i] = ROLE == 2 && row == 26;
#pragma unroll
      for (int e = 0; e < 4; ++e) {
        const int q = 4 * k + e, m = q / 13, p = q - 13 * m;
        const u32 nat = row == 26 ? lb + L_V + (169 * h2 + q) * 8 : lb + (row * 338 + 169 * h2 + q) * 4;
        const u32 trn = lb + (((13 * h2 + m) * 26 + row) * 13 + p) * 4;
        ca[i][e] = (q > 168 || (ROLE == 3 && row == 26)) ? 0x100000u : ROLE == 3 ? trn : nat;
        PIN(ca[i][e]);
      }
      PIN(dst[i]);
    }
    asm volatile("s_waitcnt vmcnt(0)" ::: "memory");
  } else front();
  asm volatile("s_waitcnt vmcnt(10)" : "+v"(ep), "+v"(wf0), "+v"(wf1) :: "memory");
  u32 mv = 0, mw = 0;
#pragma unroll
  for (int i = 0; i < 5; ++i) mw = pkmax(mw, absmax8(lds128(own + i * 1024)));
  ep = make_tag(ep);
  if (hh) wf1 = u32x4{wf1[3], 0u, 0u, 0u};
  if (ROLE == 2) {
    BAR_LG();
    float xv[3];
#pragma unroll
    for (int i = 0; i < 3; ++i) {
      f32x16 d;
#pragma unroll
      for (int e = 0; e < 16; ++e) d[e] = 0.f;
      d = __builtin_amdgcn_mfma_f32_32x32x16_bf16(__builtin_bit_cast(bf16x8, wf0), cat8(ldstr(ta0 + i * 64), ldstr(ta0 + 5408 + i * 64)), d, 0, 0, 0);
      d = __builtin_amdgcn_mfma_f32_32x32x16_bf16(__builtin_bit_cast(bf16x8, wf1), cat8(ldstr(ta2 + i * 64), ldstr(ta3 + i * 64)), d, 0, 0, 0);
      xv[i] = (wave * 3 + i) * 32 + r < 676 ? d[0] : 0.f;
    }
#pragma unroll
    for (int i = 0; i < 3; ++i) { mv = max(mv, __float_as_uint(xv[i]) & 0x7fffffffu); *(LDS_AS float*)(size_t)(lb + L_V + ((wave * 3 + i) * 32 + r) * 4) = xv[i]; }
  }
  *(LDS_AS u32*)(size_t)(lb + L_RED + wave * 4) = wave_max2(max(mw & 0xffffu, mw >> 16) | (mv & 0xffff0000u));
  BAR_LG();
  if (wave >= 4) return;
  const u32x4 t0 = lds128(lb + L_RED), t1 = lds128(lb + L_RED + 16);
  u32 c0[2][4], c1[2][4];
#pragma unroll
  for (int e = 0; e < 8; ++e) { c0[e >> 2][e & 3] = lds32(ca[e >> 2][e & 3]); c1[e >> 2][e & 3] = ROLE == 2 ? lds32(ca[e >> 2][e & 3] + 4) : 0u; }
  const u32 mm = pkmax(pkmax(pkmax(t0[0], t0[1]), pkmax(t0[2], t0[3])), pkmax(pkmax(t1[0], t1[1]), pkmax(t1[2], t1[3])));
  const int aw = expo16(mm & 0xffffu) - 15, av = expo16(mm >> 16) - 15;
  const __amdgpu_buffer_rsrc_t rs = __builtin_amdgcn_make_buffer_rsrc(a.ws, 0, WS_USED, 0x00020000);
#pragma unroll
  for (int i = 0; i < 2; ++i) {
    u32x4 z;
#pragma unroll
    for (int e = 0; e < 4; ++e) {
      const float x = vr[i] ? __uint_as_float(c0[i][e]) : bf2f(c0[i][e]), y = vr[i] ? __uint_as_float(c1[i][e]) : bfhi(c0[i][e]);
      z[e] = pack_h2(ldexpf(x, vr[i] ? -av : -aw), ldexpf(y, vr[i] ? -av : -aw));
    }
    store16_dev(rs, dst[i], z);
  }
  if (tid == 0) __hip_atomic_store((GLB_AS u64*)(a.ws + WS_CST + (layer - 1) * 8), (u64)(u32)aw | ((u64)(u32)av << 32), RLX_AGENT);
  publish(a, lb + L_RED + 32, 4, ep, pid, lane);
}

DI void wait_words(const char* base, int stride, int n, u64 tag, int lane) {
  const GLB_AS u64* p = (const GLB_AS u64*)(base + min(lane, n - 1) * stride);
  _Pragma("nounroll") for (u32 spins = 0; spins < SPIN_CAP; ++spins) {
    if (__all(__hip_atomic_load(p, RLX_AGENT) == tag)) break;
    __builtin_amdgcn_s_sleep(2);
  }
  asm volatile("" ::: "memory");
}
typedef __attribute__((ext_vector_type(16))) unsigned int u32x16;
__global__ __launch_bounds__(512) void xdeepfm_kernel(Args) {
  __shared__ __attribute__((aligned(512))) char smem[160 * 1024];
  u32x16 ka0; u32x4 ka1; int wave, x, y;
  const int tid = threadIdx.x, bid = blockIdx.x;
  asm volatile(
      "s_load_dwordx16 s[36:51], %[kp], 0x0\n"
      "s_load_dwordx4 s[52:55], %[kp], 0x40\n"
      "v_readfirstlane_b32 s56, %[tid]\n"
      "v_and_b32 v1, 63, %[tid]\n"
      "s_lshr_b32 s58, %[bid], 3\n"
      "s_lshr_b32 s56, s56, 6\n"
      "s_cmp_lt_u32 s56, 6\n"
      "s_cbranch_scc1 2f\n"
      "s_getpc_b64 s[68:69]\n"
      "3:\n"
      "s_lshl1_add_u32 s67, s58, s56\n"
      "s_lshl_b32 s67, s67, 10\n"
      "s_sub_u32 s67, s67, 6144\n"
      "v_lshl_add_u32 v2, v1, 4, s67\n"
      "s_lshl_b32 s67, s56, 10\n"
      "s_add_u32 m0, s67, 154624\n"
      "v_min_u32 v2, max(xdf_e0-3b, xdf_e1-3b, xdf_e2-3b, xdf_e3-3b, xdf_e4-3b, xdf_e5-3b)-16, v2\n"
      "global_load_lds_dwordx4 v2, s[68:69]\n"
      "2:\n"
      "s_and_b32 s57, %[bid], 7\n"
      "s_cbranch_scc1 1f\n"
      "s_mul_i32 s67, s56, 4352\n"
      "s_min_u32 s67, s67, 30032\n"
      "s_mul_i32 s68, s58, 13\n"
      "s_sub_i32 s68, s68, 144\n"
      "s_ashr_i32 s68, s68, 6\n"
      "s_max_i32 s68, s68, 0\n"
      "s_cmp_lt_u32 s58, 8\n"
      "s_cselect_b32 s68, 5, s68\n"
      "s_mul_i32 s68, s68, 35152\n"
      "s_add_u32 s68, s68, s67\n"
      "v_lshl_add_u32 v2, v1, 4, s68\n"
      "s_add_u32 m0, s67, 40960\n"
      "v_add_u32 v3, 4096, v2\n"
      "s_waitcnt lgkmcnt(0)\n"
      "global_load_lds_dwordx4 v2, s[40:41]\n"
      "global_load_lds_dwordx4 v2, s[40:41] offset:1024\n"
      "global_load_lds_dwordx4 v2, s[40:41] offset:2048\n"
      "global_load_lds_dwordx4 v2, s[40:41] offset:3072\n"
      "s_add_u32 m0, s67, 45056\n"
      "s_nop 0\n"
      "global_load_lds_dwordx4 v3, s[40:41]\n"
      "1:\n"
      : "=&{s[36:51]}"(ka0), "=&{s[52:55]}"(ka1), "=&{s56}"(wave), "=&{s57}"(x), "=&{s58}"(y)
      : [kp] "s"(__builtin_amdgcn_kernarg_segment_ptr()), [tid] "{v0}"(tid), [bid] "s"(bid)
      : "memory", "scc", "s67", "s68", "s69", "s70", "s71", "v1", "v2", "v3", "v4");
  LDS_AS char* l = (LDS_AS char*)smem;
  constexpr u32 lb = 0;
  auto args = [&]() {
    auto ptr = [&](int i) { return (char*)(i < 8 ? (u64)ka0[2 * i] | ((u64)ka0[2 * i + 1] << 32) : (u64)ka1[2 * i - 16] | ((u64)ka1[2 * i - 15] << 32)); };
    return Args{(const int*)ptr(0), (const u16*)ptr(1), (const u16*)ptr(2), (const u16*)ptr(3), (const u16*)ptr(4), (const u16*)ptr(5), (const u16*)ptr(6), (const u16*)ptr(7), (u16*)ptr(8), ptr(9)};
  };
  if (__builtin_expect(x == 0, 0)) {
    asm volatile("" : "+{s[36:51]}"(ka0), "+{s[52:55]}"(ka1));
    const Args a = args();
    const int lane = tid & 63;
    const bool dnn = wave < 4;
    const int sl = ((y - 16) * 13) >> 6;
    *(LDS_AS u32x4*)(l + L_MISC + 128) = u32x4{0u, 0u, 0u, 0u};
    auto front = [&]() {
      const u32 ib = (bid * 16 + (wave & 3) * 4) * 104, tp = min((wave & 3) * 8192, NV * 32 - 8192), tl = tp + lane * 16, to = tl ^ ((tl >> 5) & 16);
      asm volatile("s_mov_b32 m0, %6\n\ts_nop 0\n\tglobal_load_lds_dword %0, %4\n\tglobal_load_lds_dword %1, %4 offset:256\n\ts_mov_b32 m0, %7\n\ts_nop 0\n\t"
                   "global_load_lds_dwordx4 %2, %5\n\tglobal_load_lds_dwordx4 %2, %5 offset:1024\n\tglobal_load_lds_dwordx4 %2, %5 offset:2048\n\tglobal_load_lds_dwordx4 %2, %5 offset:3072\n\t"
                   "s_add_u32 m0, %7, 4096\n\ts_nop 0\n\t"
                   "global_load_lds_dwordx4 %3, %5\n\tglobal_load_lds_dwordx4 %3, %5 offset:1024\n\tglobal_load_lds_dwordx4 %3, %5 offset:2048\n\tglobal_load_lds_dwordx4 %3, %5 offset:3072"
                   :: "v"(ib + lane * 4), "v"(ib + min(lane, 39) * 4), "v"(to), "v"(to + 4096), "s"(a.idx), "s"(a.table),
                      "s"(lb + L_IDX + 14336 + (wave & 3) * 512), "s"(lb + L_TAB + tp) : "memory", "scc");
    };
    const u32 own = lb + L_W1I + min(wave * 4352, 35152 - 5120) + lane * 16;
    if (y < 8) { prepq<0>(a, lb + L_W1I, own, y, tid, lane, wave, front); asm volatile("xdf_e1:"); }
    else if (y < 15) { prepq<1>(a, lb + L_W1I, own, y - 8, tid, lane, wave, front); asm volatile("xdf_e2:"); }
    else if (y < 16) { prepq<2>(a, lb + L_W1I, own, 0, tid, lane, wave, front); asm volatile("xdf_e3:"); }
    else if (y < 26) { prep<2>(a, lb + L_W1I, own, y - 16, sl + 1, y - 16 - 5 * sl, tid, lane, wave, front); asm volatile("xdf_e4:"); }
    else if (y < 31) { prep<3>(a, lb + L_W1I, own, y - 16, 3, y - 26, tid, lane, wave, front); asm volatile("xdf_e5:"); }
    else { if (!dnn) front(); BAR_VM(0); if (dnn) __builtin_amdgcn_s_sleep(DLY1); }
    asm volatile("s_waitcnt lgkmcnt(0)" ::: "memory");
  }
  u32x2 tg;
  u32x4 buf[10];
  asm volatile(
      "s_lshl_b32 s66, s58, 2\n"
      "s_lshr_b32 s66, 0x83965410, s66\n"
      "s_and_b32 s66, s66, 15\n"
      "s_lshl_b32 s66, s66, 3\n"
      "s_load_dwordx2 s[72:73], %[kp], s66\n"
      "v_mov_b32 v250, 0\n"
      "v_and_b32 v253, 63, %[tid]\n"
      "s_lshl_b32 s68, s56, 12\n"
      "s_min_u32 s68, s68, 27904\n"
      "s_sub_u32 s59, s57, 1\n"
      "v_lshl_add_u32 v254, v253, 4, s68\n"
      "s_and_b32 s60, s56, 3\n"
      "v_lshrrev_b32 v255, 5, v254\n"
      "s_max_i32 s67, s59, 0\n"
      "v_bitop3_b32 v251, v255, v254, 16 bitop3:0x6c\n"
      "s_add_u32 s68, s68, 94208\n"
      "s_cmp_gt_u32 s56, 3\n"
      "s_cbranch_scc1 xc_pre\n"
      "s_mul_i32 s61, s56, 3584\n"
      "v_and_b32 v242, 31, v253\n"
      "s_add_u32 s61, s61, 126976\n"
      "s_cmp_lt_i32 s59, 0\n"
      "s_cbranch_scc1 1f\n"
      "s_mul_i32 s69, s58, 13312\n"
      "s_mul_i32 s66, s56, 3328\n"
      "s_add_u32 s69, s69, s66\n"
      "s_lshl_b32 s66, s67, 7\n"
      "v_lshl_add_u32 v245, v253, 4, s69\n"
      "v_lshl_add_u32 v246, v253, 2, s69\n"
      "v_lshl_add_u32 v247, v242, 2, s66\n"
      "s_mov_b32 m0, s61\n"
      "v_min_u32 v247, 828, v247\n"
      "s_waitcnt lgkmcnt(0)\n"
      "global_load_ubyte v249, v250, s[72:73]\n"
      "global_load_lds_dwordx4 v245, s[36:37]\n"
      "global_load_lds_dwordx4 v245, s[36:37] offset:1024\n"
      "global_load_lds_dwordx4 v245, s[36:37] offset:2048\n"
      "global_load_lds_dword v246, s[36:37] offset:3072\n"
      "s_add_u32 m0, s61, 3328\n"
      "s_mov_b32 exec_hi, 0\n"
      "global_load_lds_dword v247, s[46:47]\n"
      "s_not_b64 exec, exec\n"
      "global_load_lds_dword v247, s[48:49]\n"
      "s_mov_b64 exec, -1\n"
      "s_mul_i32 s69, s56, 7\n"
      "v_min_u32 v254, 6, v253\n"
      "s_mul_i32 s63, s58, 10816\n"
      "v_add_u32 v254, s69, v254\n"
      "s_lshl_b32 s69, s67, 7\n"
      "v_min_u32 v254, 25, v254\n"
      "s_add_u32 s63, s63, s69\n"
      "v_lshrrev_b32 v255, 1, v254\n"
      "s_cmp_lt_i32 s59, 6\n"
      "v_mul_u32_u24 v255, 832, v255\n"
      "s_cselect_b32 s69, 1, 0\n"
      "v_and_b32 v254, s69, v254\n"
      "v_lshl_add_u32 v254, v254, 6, v255\n"
      "v_add_u32 v243, s63, v254\n"
      "s_mov_b32 m0, 163072\n"
      "s_nop 0\n"
      "global_load_lds_dword v243, s[44:45]\n"
      "s_mov_b32 m0, s68\n"
      "s_mov_b64 exec, -1\n"
      "global_load_lds_dwordx4 v251, s[38:39] offset:0\n"
      "global_load_lds_dwordx4 v251, s[38:39] offset:1024\n"
      "global_load_lds_dwordx4 v251, s[38:39] offset:2048\n"
      "global_load_lds_dwordx4 v251, s[38:39] offset:3072\n"
      "v_mov_b32 v40, 0\n"
      "v_mov_b32 v41, 0\n"
      "v_mov_b32 v42, 0\n"
      "v_mov_b32 v43, 0\n"
      "v_mov_b32 v46, 159744\n"
      "ds_write_b128 v46, v[40:43] offset:128\n"
      "1:\n"
      "v_mov_b32 v47, 0\n"
      "global_load_dwordx2 v[44:45], v47, s[54:55] sc1\n"
      "v_lshrrev_b32 v243, 5, v253\n"
      "v_mul_u32_u24 v254, 104, v242\n"
      "s_mov_b32 s66, 94208\n"
      "v_add_u32 v1, s61, v254\n"
      "s_add_u32 s69, s61, 3328\n"
      "v_lshl_add_u32 v2, v243, 4, s66\n"
      "s_mov_b32 s66, 40960\n"
      "v_lshl_add_u32 v3, v243, 5, s69\n"
      "v_lshl_add_u32 v254, v243, 9, s66\n"
      "v_bfe_u32 v255, v253, 2, 2\n"
      "v_lshl_add_u32 v254, v255, 6, v254\n"
      "v_and_b32 v255, 1, v253\n"
      "v_lshl_add_u32 v254, v255, 5, v254\n"
      "v_bfe_u32 v244, v253, 4, 1\n"
      "v_lshl_add_u32 v254, v244, 4, v254\n"
      "v_bfe_u32 v255, v253, 1, 1\n"
      "v_lshl_add_u32 v4, v255, 3, v254\n"
      "s_lshl_b32 s66, s57, 12\n"
      "s_add_u32 s66, s66, 196608\n"
      "v_min_u32 v254, 14, v253\n"
      "v_lshl_add_u32 v6, v254, 7, s66\n"
      "s_mul_i32 s66, s56, 10240\n"
      "s_add_u32 s66, s66, 60224\n"
      "v_lshl_add_u32 v11, v253, 4, s66\n"
      "s_lshl_b32 s66, s67, 16\n"
      "s_lshl_b32 s69, s58, 11\n"
      "s_add_u32 s66, s66, s69\n"
      "s_lshl_b32 s69, s56, 9\n"
      "s_add_u32 s66, s66, s69\n"
      "s_add_u32 s66, s66, 262144\n"
      "v_lshl_add_u32 v254, v242, 4, s66\n"
      "v_lshl_add_u32 v10, v243, 3, v254\n"
      "s_cmp_lt_i32 s59, 6\n"
      "s_cselect_b32 s87, -1, 0\n"
      "s_and_b32 s66, s87, 64\n"
      "s_add_u32 s64, s44, s66\n"
      "s_addc_u32 s65, s45, 0\n"
      "v_min_u32 v254, 9, v253\n"
      "s_mov_b32 s66, 160000\n"
      "v_lshl_add_u32 v246, v254, 4, s66\n"
      "v_lshlrev_b32 v254, 7, v254\n"
      "v_add_u32 v245, 55296, v254\n"
      "v_mov_b32 v240, 0xbdd2d3e8\n"
      "v_mov_b32 v241, 0xc0135761\n"
      "s_mul_i32 s66, s60, 13312\n"
      "v_lshrrev_b32 v254, 2, v253\n"
      "s_lshl_b32 s69, s67, 7\n"
      "v_mul_u32_u24 v254, 832, v254\n"
      "s_add_u32 s66, s66, s69\n"
      "v_and_b32 v255, 3, v253\n"
      "s_lshl_b32 s62, s60, 10\n"
      "v_lshl_add_u32 v254, v255, 4, v254\n"
      "s_add_u32 s62, s62, 40960\n"
      "v_add_u32 v9, s66, v254\n"
      "s_lshl_b32 s66, s60, 3\n"
      "v_lshl_add_u32 v254, v243, 2, v244\n"
      "s_min_u32 s66, s66, 19\n"
      "v_bfe_u32 v255, v253, 3, 1\n"
      "v_lshl_add_u32 v254, v255, 1, v254\n"
      "v_add_u32 v254, s66, v254\n"
      "v_and_b32 v255, 7, v253\n"
      "v_lshlrev_b32 v255, 4, v255\n"
      "v_lshl_add_u32 v5, v254, 11, v255\n"
      "v_bfe_u32 v255, v253, 1, 2\n"
      "v_mul_u32_u24 v255, 432, v255\n"
      "v_lshl_add_u32 v254, v254, 4, v255\n"
      "v_and_b32 v255, 1, v253\n"
      "v_lshl_add_u32 v8, v255, 3, v254\n"
      "v_bfe_u32 v255, v253, 2, 1\n"
      "v_mul_u32_u24 v255, 864, v255\n"
      "v_add_u32 v7, v8, v255\n"
      "s_cmp_gt_u32 s60, 1\n"
      "s_cselect_b32 s63, 22, 24\n"
      "s_mov_b32 s70, 0\n"
      "s_mov_b32 s71, -1\n"
      "s_add_u32 s92, s54, 786432\n"
      "s_addc_u32 s93, s55, 0\n"
      "s_add_u32 s94, s92, 65536\n"
      "s_addc_u32 s95, s93, 0\n"
      "s_cmp_lt_i32 s59, 0\n"
      "s_cbranch_scc1 p1_nodnn\n"
      "s_setprio 2\n"
      "s_waitcnt vmcnt(6)\n"
      "ds_read_b64 v[12:13], v1 offset:0\n"
      "ds_read_b64 v[14:15], v1 offset:8\n"
      "ds_read_b64 v[16:17], v1 offset:16\n"
      "ds_read_b64 v[18:19], v1 offset:24\n"
      "ds_read_b64 v[20:21], v1 offset:32\n"
      "ds_read_b64 v[22:23], v1 offset:40\n"
      "ds_read_b64 v[24:25], v1 offset:48\n"
      "ds_read_b64 v[26:27], v1 offset:56\n"
      "ds_read_b64 v[28:29], v1 offset:64\n"
      "ds_read_b64 v[30:31], v1 offset:72\n"
      "ds_read_b64 v[32:33], v1 offset:80\n"
      "ds_read_b64 v[34:35], v1 offset:88\n"
      "ds_read_b64 v[36:37], v1 offset:96\n"
      "ds_read_b128 v[56:59], v3 offset:0\n"
      "s_waitcnt lgkmcnt(14)\n"
      "ds_read_b128 v[60:63], v3 offset:16\n"
      "s_waitcnt lgkmcnt(14)\n"
      "v_lshl_add_u32 v40, v12, 5, v2\n"
      "v_bitop3_b32 v12, v12, v40, 16 bitop3:0x6c\n"
      "v_lshl_add_u32 v41, v13, 5, v2\n"
      "v_bitop3_b32 v13, v13, v41, 16 bitop3:0x6c\n"
      "s_waitcnt lgkmcnt(13)\n"
      "v_lshl_add_u32 v42, v14, 5, v2\n"
      "v_bitop3_b32 v14, v14, v42, 16 bitop3:0x6c\n"
      "v_lshl_add_u32 v43, v15, 5, v2\n"
      "v_bitop3_b32 v15, v15, v43, 16 bitop3:0x6c\n"
      "s_waitcnt lgkmcnt(12)\n"
      "v_lshl_add_u32 v40, v16, 5, v2\n"
      "v_bitop3_b32 v16, v16, v40, 16 bitop3:0x6c\n"
      "v_lshl_add_u32 v41, v17, 5, v2\n"
      "v_bitop3_b32 v17, v17, v41, 16 bitop3:0x6c\n"
      "s_waitcnt lgkmcnt(11)\n"
      "v_lshl_add_u32 v42, v18, 5, v2\n"
      "v_bitop3_b32 v18, v18, v42, 16 bitop3:0x6c\n"
      "v_lshl_add_u32 v43, v19, 5, v2\n"
      "v_bitop3_b32 v19, v19, v43, 16 bitop3:0x6c\n"
      "ds_read_b128 v[72:75], v3 offset:64\n"
      "ds_read_b128 v[76:79], v3 offset:80\n"
      "ds_read_b128 v[88:91], v3 offset:128\n"
      "ds_read_b128 v[92:95], v3 offset:144\n"
      "s_waitcnt lgkmcnt(14)\n"
      "ds_read_b128 v[104:107], v3 offset:192\n"
      "s_waitcnt lgkmcnt(14)\n"
      "ds_read_b128 v[108:111], v3 offset:208\n"
      "s_waitcnt lgkmcnt(7)\n"
      "v_lshlrev_b32 v48, 16, v56\n"
      "v_and_b32 v49, 0xffff0000, v56\n"
      "v_lshlrev_b32 v50, 16, v57\n"
      "v_and_b32 v51, 0xffff0000, v57\n"
      "v_lshlrev_b32 v52, 16, v58\n"
      "v_and_b32 v53, 0xffff0000, v58\n"
      "v_lshlrev_b32 v54, 16, v59\n"
      "v_and_b32 v55, 0xffff0000, v59\n"
      "s_waitcnt lgkmcnt(6)\n"
      "v_lshlrev_b32 v56, 16, v60\n"
      "v_and_b32 v57, 0xffff0000, v60\n"
      "v_lshlrev_b32 v58, 16, v61\n"
      "v_and_b32 v59, 0xffff0000, v61\n"
      "v_lshlrev_b32 v60, 16, v62\n"
      "v_and_b32 v61, 0xffff0000, v62\n"
      "v_lshlrev_b32 v62, 16, v63\n"
      "v_and_b32 v63, 0xffff0000, v63\n"
      "v_lshl_add_u32 v40, v20, 5, v2\n"
      "v_bitop3_b32 v20, v20, v40, 16 bitop3:0x6c\n"
      "v_lshl_add_u32 v41, v21, 5, v2\n"
      "v_bitop3_b32 v21, v21, v41, 16 bitop3:0x6c\n"
      "v_lshl_add_u32 v42, v22, 5, v2\n"
      "v_bitop3_b32 v22, v22, v42, 16 bitop3:0x6c\n"
      "v_lshl_add_u32 v43, v23, 5, v2\n"
      "v_bitop3_b32 v23, v23, v43, 16 bitop3:0x6c\n"
      "v_lshl_add_u32 v40, v24, 5, v2\n"
      "v_bitop3_b32 v24, v24, v40, 16 bitop3:0x6c\n"
      "v_lshl_add_u32 v41, v25, 5, v2\n"
      "v_bitop3_b32 v25, v25, v41, 16 bitop3:0x6c\n"
      "v_lshl_add_u32 v42, v26, 5, v2\n"
      "v_bitop3_b32 v26, v26, v42, 16 bitop3:0x6c\n"
      "v_lshl_add_u32 v43, v27, 5, v2\n"
      "v_bitop3_b32 v27, v27, v43, 16 bitop3:0x6c\n"
      "v_lshl_add_u32 v40, v28, 5, v2\n"
      "v_bitop3_b32 v28, v28, v40, 16 bitop3:0x6c\n"
      "v_lshl_add_u32 v41, v29, 5, v2\n"
      "v_bitop3_b32 v29, v29, v41, 16 bitop3:0x6c\n"
      "v_lshl_add_u32 v42, v30, 5, v2\n"
      "v_bitop3_b32 v30, v30, v42, 16 bitop3:0x6c\n"
      "v_lshl_add_u32 v43, v31, 5, v2\n"
      "v_bitop3_b32 v31, v31, v43, 16 bitop3:0x6c\n"
      "v_lshl_add_u32 v40, v32, 5, v2\n"
      "v_bitop3_b32 v32, v32, v40, 16 bitop3:0x6c\n"
      "v_lshl_add_u32 v41, v33, 5, v2\n"
      "v_bitop3_b32 v33, v33, v41, 16 bitop3:0x6c\n"
      "v_lshl_add_u32 v42, v34, 5, v2\n"
      "v_bitop3_b32 v34, v34, v42, 16 bitop3:0x6c\n"
      "v_lshl_add_u32 v43, v35, 5, v2\n"
      "v_bitop3_b32 v35, v35, v43, 16 bitop3:0x6c\n"
      "v_lshl_add_u32 v40, v36, 5, v2\n"
      "v_bitop3_b32 v36, v36, v40, 16 bitop3:0x6c\n"
      "v_lshl_add_u32 v41, v37, 5, v2\n"
      "v_bitop3_b32 v37, v37, v41, 16 bitop3:0x6c\n"
      "v_mov_b32 v40, 159744\n"
      "v_mov_b32 v41, 1\n"
      "s_branch xl_body\n"
      "xc_meet:\n"
      "v_mov_b32 v254, 159744\n"
      "v_mov_b32 v255, 1\n"
      "s_mov_b32 s88, 0x400000\n"
      "s_waitcnt vmcnt(4)\n"
      "s_mov_b64 exec, 1\n"
      "ds_add_u32 v254, v255 offset:128\n"
      "s_mov_b64 exec, -1\n"
      "4:\n"
      "ds_read_b32 v255, v254 offset:128\n"
      "s_sub_u32 s88, s88, 1\n"
      "s_waitcnt lgkmcnt(0)\n"
      "s_cbranch_scc1 xc_met\n"
      "v_cmp_gt_u32 vcc, 4, v255\n"
      "s_cbranch_vccnz 4b\n"
      "s_branch xc_met\n"
      "xc_pre:\n"
      "s_lshl_b32 s61, s60, 9\n"
      "s_add_u32 s61, s61, 141312\n"
      "s_cmp_lt_i32 s59, 0\n"
      "s_cbranch_scc1 1f\n"
      "s_mul_i32 s66, s60, 13312\n"
      "v_lshrrev_b32 v254, 2, v253\n"
      "s_lshl_b32 s69, s67, 7\n"
      "v_mul_u32_u24 v254, 832, v254\n"
      "s_add_u32 s66, s66, s69\n"
      "v_and_b32 v255, 3, v253\n"
      "s_lshl_b32 s62, s60, 10\n"
      "v_lshl_add_u32 v254, v255, 4, v254\n"
      "s_add_u32 s62, s62, 40960\n"
      "v_add_u32 v1, s66, v254\n"
      "s_mul_i32 s69, %[bid], 1664\n"
      "s_mul_i32 s66, s60, 416\n"
      "s_add_u32 s69, s69, s66\n"
      "v_min_u32 v254, 39, v253\n"
      "v_lshl_add_u32 v18, v253, 2, s69\n"
      "s_mov_b32 m0, s61\n"
      "v_lshl_add_u32 v19, v254, 2, s69\n"
      "s_waitcnt lgkmcnt(0)\n"
      "global_load_ubyte v249, v250, s[72:73]\n"
      "global_load_lds_dword v18, s[36:37]\n"
      "global_load_lds_dword v19, s[36:37] offset:256\n"
      "s_mov_b32 m0, s62\n"
      "s_nop 0\n"
      "global_load_lds_dwordx4 v1, s[44:45]\n"
      "s_mov_b32 m0, s68\n"
      "s_mov_b64 exec, -1\n"
      "global_load_lds_dwordx4 v251, s[38:39] offset:0\n"
      "global_load_lds_dwordx4 v251, s[38:39] offset:1024\n"
      "global_load_lds_dwordx4 v251, s[38:39] offset:2048\n"
      "global_load_lds_dwordx4 v251, s[38:39] offset:3072\n"
      "s_add_u32 m0, s62, 4096\n"
      "v_add_u32 v58, 53248, v1\n"
      "global_load_lds_dwordx4 v58, s[44:45]\n"
      "1:\n"
      "v_lshrrev_b32 v16, 5, v253\n"
      "v_and_b32 v20, 15, v253\n"
      "v_min_u32 v254, 12, v20\n"
      "v_mad_u32_u24 v254, v16, 13, v254\n"
      "v_lshlrev_b32 v5, 1, v254\n"
      "v_bfe_u32 v254, v253, 3, 2\n"
      "v_mul_u32_u24 v254, 104, v254\n"
      "s_mov_b32 s66, 94208\n"
      "v_add_u32 v4, s61, v254\n"
      "v_and_b32 v255, 7, v253\n"
      "v_lshl_add_u32 v2, v16, 6, v4\n"
      "v_lshl_add_u32 v3, v255, 2, s66\n"
      "s_mov_b32 s66, 160000\n"
      "s_lshl_b32 s69, s60, 12\n"
      "v_lshl_add_u32 v14, v16, 6, s66\n"
      "s_add_u32 s69, s69, 143360\n"
      "v_lshl_add_u32 v6, v20, 2, v14\n"
      "v_min_u32 v254, 9, v253\n"
      "v_lshl_add_u32 v11, v254, 4, s66\n"
      "v_lshlrev_b32 v254, 7, v254\n"
      "v_add_u32 v9, 55296, v254\n"
      "v_bfe_u32 v17, v253, 4, 1\n"
      "v_lshl_add_u32 v254, v20, 5, s69\n"
      "v_lshl_add_u32 v254, v17, 11, v254\n"
      "v_lshl_add_u32 v7, v16, 10, v254\n"
      "v_and_b32 v254, 31, v253\n"
      "v_lshl_add_u32 v12, v254, 4, s69\n"
      "s_lshl_b32 s66, s60, 4\n"
      "s_add_u32 s66, s66, 159808\n"
      "v_lshrrev_b32 v254, 4, v253\n"
      "v_lshl_add_u32 v15, v254, 2, s66\n"
      "v_bfe_u32 v254, v253, 2, 3\n"
      "v_or_b32 v254, 0x0c0c0c00, v254\n"
      "v_mov_b32 v255, 0x04120e00\n"
      "s_mov_b32 s66, 0x1a0c0816\n"
      "v_perm_b32 v254, s66, v255, v254\n"
      "v_and_b32 v255, 3, v253\n"
      "v_add_u32 v254, v254, v255\n"
      "v_mul_u32_u24 v255, 432, v16\n"
      "v_lshl_add_u32 v254, v254, 4, v255\n"
      "s_mov_b32 vcc_lo, 0x13ffffff\n"
      "s_mov_b32 vcc_hi, 0x13ffffff\n"
      "v_mov_b32 v255, 0x100000\n"
      "v_cndmask_b32 v13, v255, v254, vcc\n"
      "s_lshl_b32 s66, %[bid], 5\n"
      "s_add_u32 s74, s52, s66\n"
      "s_addc_u32 s75, s53, 0\n"
      "s_waitcnt lgkmcnt(0)\n"
      "s_mov_b32 s72, 0x10001\n"
      "s_mov_b32 s73, 0x10001\n"
      "s_mov_b32 s76, 0x0b000b00\n"
      "s_mov_b32 s77, 0x7fff7fff\n"
      "s_mov_b32 s78, 0x05040100\n"
      "s_mov_b32 s79, 0x07060302\n"
      "s_mov_b32 s80, 0x82800000\n"
      "s_mov_b32 s81, 0x0d800000\n"
      "s_cmp_lt_i32 s59, 0\n"
      "s_cselect_b32 s86, 4, 8\n"
      "s_lshl_b32 s66, s60, 3\n"
      "v_lshl_add_u32 v254, v16, 2, v17\n"
      "s_min_u32 s66, s66, 19\n"
      "v_bfe_u32 v255, v253, 3, 1\n"
      "v_lshl_add_u32 v254, v255, 1, v254\n"
      "v_add_u32 v254, s66, v254\n"
      "v_and_b32 v255, 7, v253\n"
      "v_lshlrev_b32 v255, 4, v255\n"
      "v_lshl_add_u32 v8, v254, 11, v255\n"
      "v_bfe_u32 v255, v253, 1, 2\n"
      "v_mul_u32_u24 v255, 432, v255\n"
      "v_lshl_add_u32 v254, v254, 4, v255\n"
      "v_and_b32 v255, 1, v253\n"
      "v_lshl_add_u32 v252, v255, 3, v254\n"
      "v_bfe_u32 v255, v253, 2, 1\n"
      "v_mul_u32_u24 v255, 864, v255\n"
      "v_add_u32 v10, v252, v255\n"
      "s_cmp_gt_u32 s60, 1\n"
      "s_cselect_b32 s63, 22, 24\n"
      "s_mov_b32 s70, 0\n"
      "s_mov_b32 s71, -1\n"
      "s_add_u32 s92, s54, 786432\n"
      "s_addc_u32 s93, s55, 0\n"
      "s_add_u32 s94, s92, 65536\n"
      "s_addc_u32 s95, s93, 0\n"
      "s_cmp_ge_i32 s59, 0\n"
      "s_cbranch_scc1 3f\n"
      "v_mov_b32 v59, 0\n"
      "global_load_dwordx2 v[56:57], v59, s[54:55] sc1\n"
      "global_load_ushort v78, v5, s[42:43] offset:156\n"
      "s_mov_b32 m0, 162816\n"
      "s_nop 0\n"
      "global_load_lds_ushort v59, s[74:75]\n"
      "global_load_lds_ushort v59, s[50:51]\n"
      "s_sub_u32 s74, s74, 79904\n"
      "s_subb_u32 s75, s75, 0\n"
      "3:\n"
      "s_setprio 2\n"
      "s_waitcnt vmcnt(12)\n"
      "s_cmp_lt_i32 s59, 0\n"
      "s_cbranch_scc1 6f\n"
      "s_waitcnt vmcnt(6)\n"
      "6:\n"
      "ds_read_b64 v[24:25], v2 offset:0\n"
      "ds_read_b64 v[26:27], v2 offset:8\n"
      "ds_read_b64 v[28:29], v2 offset:16\n"
      "ds_read_b64 v[30:31], v2 offset:24\n"
      "ds_read_b64 v[32:33], v2 offset:32\n"
      "ds_read_b64 v[34:35], v4 offset:40\n"
      "ds_read_b64 v[36:37], v4 offset:48\n"
      "ds_read_b64 v[38:39], v4 offset:56\n"
      "s_waitcnt lgkmcnt(7)\n"
      "v_lshl_add_u32 v52, v24, 5, v3\n"
      "v_bitop3_b32 v24, v24, v52, 16 bitop3:0x6c\n"
      "v_lshl_add_u32 v53, v25, 5, v3\n"
      "v_bitop3_b32 v25, v25, v53, 16 bitop3:0x6c\n"
      "s_waitcnt lgkmcnt(6)\n"
      "v_lshl_add_u32 v54, v26, 5, v3\n"
      "v_bitop3_b32 v26, v26, v54, 16 bitop3:0x6c\n"
      "v_lshl_add_u32 v55, v27, 5, v3\n"
      "v_bitop3_b32 v27, v27, v55, 16 bitop3:0x6c\n"
      "s_waitcnt lgkmcnt(5)\n"
      "v_lshl_add_u32 v52, v28, 5, v3\n"
      "v_bitop3_b32 v28, v28, v52, 16 bitop3:0x6c\n"
      "v_lshl_add_u32 v53, v29, 5, v3\n"
      "v_bitop3_b32 v29, v29, v53, 16 bitop3:0x6c\n"
      "s_waitcnt lgkmcnt(4)\n"
      "v_lshl_add_u32 v54, v30, 5, v3\n"
      "v_bitop3_b32 v30, v30, v54, 16 bitop3:0x6c\n"
      "v_lshl_add_u32 v55, v31, 5, v3\n"
      "v_bitop3_b32 v31, v31, v55, 16 bitop3:0x6c\n"
      "s_waitcnt lgkmcnt(3)\n"
      "v_lshl_add_u32 v52, v32, 5, v3\n"
      "v_bitop3_b32 v32, v32, v52, 16 bitop3:0x6c\n"
      "v_lshl_add_u32 v53, v33, 5, v3\n"
      "v_bitop3_b32 v33, v33, v53, 16 bitop3:0x6c\n"
      "s_waitcnt lgkmcnt(2)\n"
      "v_lshl_add_u32 v54, v34, 5, v3\n"
      "v_bitop3_b32 v34, v34, v54, 16 bitop3:0x6c\n"
      "v_lshl_add_u32 v55, v35, 5, v3\n"
      "v_bitop3_b32 v35, v35, v55, 16 bitop3:0x6c\n"
      "s_waitcnt lgkmcnt(1)\n"
      "v_lshl_add_u32 v52, v36, 5, v3\n"
      "v_bitop3_b32 v36, v36, v52, 16 bitop3:0x6c\n"
      "v_lshl_add_u32 v53, v37, 5, v3\n"
      "v_bitop3_b32 v37, v37, v53, 16 bitop3:0x6c\n"
      "s_waitcnt lgkmcnt(0)\n"
      "v_lshl_add_u32 v54, v38, 5, v3\n"
      "v_bitop3_b32 v38, v38, v54, 16 bitop3:0x6c\n"
      "v_lshl_add_u32 v55, v39, 5, v3\n"
      "v_bitop3_b32 v39, v39, v55, 16 bitop3:0x6c\n"
      "s_cmp_lt_i32 s59, 0\n"
      "s_cbranch_scc1 2f\n"
      "s_add_u32 m0, s62, 8192\n"
      "v_add_u32 v58, 106496, v1\n"
      "global_load_lds_dwordx4 v58, s[44:45]\n"
      "s_add_u32 m0, s62, 12288\n"
      "v_add_u32 v58, 159744, v1\n"
      "global_load_lds_dwordx4 v58, s[44:45]\n"
      "s_add_u32 m0, s62, 16384\n"
      "v_add_u32 v58, 212992, v1\n"
      "global_load_lds_dwordx4 v58, s[44:45]\n"
      "s_add_u32 m0, s62, 20480\n"
      "v_add_u32 v58, 266240, v1\n"
      "global_load_lds_dwordx4 v58, s[44:45]\n"
      "s_mul_i32 s88, s63, 13312\n"
      "s_lshl_b32 m0, s63, 10\n"
      "v_add_u32 v58, s88, v1\n"
      "s_add_u32 m0, m0, s62\n"
      "s_add_u32 m0, m0, 0\n"
      "s_nop 0\n"
      "global_load_lds_dwordx4 v58, s[44:45]\n"
      "2:\n"
      "s_cmp_lt_i32 s59, 0\n"
      "s_cbranch_scc1 xc_meet\n"
      "s_waitcnt vmcnt(5)\n"
      "s_barrier\n"
      "xc_met:\n"
      "ds_read_b32 v24, v24\n"
      "ds_read_b32 v25, v25\n"
      "ds_read_b32 v26, v26\n"
      "ds_read_b32 v27, v27\n"
      "ds_read_b32 v28, v28\n"
      "ds_read_b32 v29, v29\n"
      "ds_read_b32 v30, v30\n"
      "ds_read_b32 v31, v31\n"
      "ds_read_b32 v32, v32\n"
      "ds_read_b32 v33, v33\n"
      "ds_read_b32 v34, v34\n"
      "ds_read_b32 v35, v35\n"
      "ds_read_b32 v36, v36\n"
      "ds_read_b32 v37, v37\n"
      "ds_read_b32 v38, v38\n"
      "s_cmp_lt_i32 s59, 0\n"
      "s_cbranch_scc1 3f\n"
      "v_mov_b32 v59, 0\n"
      "global_load_dwordx2 v[56:57], v59, s[54:55] sc1\n"
      "global_load_ushort v78, v5, s[42:43] offset:156\n"
      "s_mov_b32 m0, 162816\n"
      "s_nop 0\n"
      "global_load_lds_ushort v59, s[74:75]\n"
      "global_load_lds_ushort v59, s[50:51]\n"
      "s_sub_u32 s74, s74, 79904\n"
      "s_subb_u32 s75, s75, 0\n"
      "3:\n"
      "s_cmp_lt_i32 s59, 0\n"
      "s_cbranch_scc1 5f\n"
      "s_waitcnt vmcnt(4)\n"
      "s_barrier\n"
      "5:\n"
      "s_waitcnt lgkmcnt(14)\n"
      "v_and_b32 v60, 0x7fff7fff, v24\n"
      "ds_read_b32 v39, v39\n"
      "s_waitcnt lgkmcnt(14)\n"
      "v_and_b32 v61, 0x7fff7fff, v25\n"
      "v_pk_max_u16 v76, v60, v61\n"
      "s_waitcnt lgkmcnt(13)\n"
      "v_and_b32 v62, 0x7fff7fff, v26\n"
      "v_pk_max_u16 v76, v76, v62\n"
      "s_waitcnt lgkmcnt(12)\n"
      "v_and_b32 v63, 0x7fff7fff, v27\n"
      "v_pk_max_u16 v76, v76, v63\n"
      "s_waitcnt lgkmcnt(11)\n"
      "v_and_b32 v64, 0x7fff7fff, v28\n"
      "v_pk_max_u16 v76, v76, v64\n"
      "s_waitcnt lgkmcnt(10)\n"
      "v_and_b32 v65, 0x7fff7fff, v29\n"
      "v_pk_max_u16 v76, v76, v65\n"
      "s_waitcnt lgkmcnt(9)\n"
      "v_and_b32 v66, 0x7fff7fff, v30\n"
      "v_pk_max_u16 v76, v76, v66\n"
      "s_waitcnt lgkmcnt(8)\n"
      "v_and_b32 v67, 0x7fff7fff, v31\n"
      "v_pk_max_u16 v76, v76, v67\n"
      "s_waitcnt lgkmcnt(7)\n"
      "v_and_b32 v68, 0x7fff7fff, v32\n"
      "v_pk_max_u16 v76, v76, v68\n"
      "s_waitcnt lgkmcnt(6)\n"
      "v_and_b32 v69, 0x7fff7fff, v33\n"
      "v_pk_max_u16 v76, v76, v69\n"
      "s_waitcnt lgkmcnt(5)\n"
      "v_and_b32 v70, 0x7fff7fff, v34\n"
      "v_pk_max_u16 v76, v76, v70\n"
      "s_waitcnt lgkmcnt(4)\n"
      "v_and_b32 v71, 0x7fff7fff, v35\n"
      "v_pk_max_u16 v76, v76, v71\n"
      "s_waitcnt lgkmcnt(3)\n"
      "v_and_b32 v72, 0x7fff7fff, v36\n"
      "v_pk_max_u16 v76, v76, v72\n"
      "s_waitcnt lgkmcnt(2)\n"
      "v_and_b32 v73, 0x7fff7fff, v37\n"
      "v_pk_max_u16 v76, v76, v73\n"
      "s_waitcnt lgkmcnt(1)\n"
      "v_and_b32 v74, 0x7fff7fff, v38\n"
      "v_pk_max_u16 v76, v76, v74\n"
      "s_waitcnt lgkmcnt(0)\n"
      "v_and_b32 v75, 0x7fff7fff, v39\n"
      "v_pk_max_u16 v76, v76, v75\n"
      "v_mov_b32 v77, v76\n"
      "s_nop 1\n"
      "v_permlane32_swap_b32 v76, v77\n"
      "v_pk_max_u16 v76, v76, v77\n"
      "v_and_b32 v76, 0x7f807f80, v76\n"
      "v_pk_sub_u16 v76, v76, s76 clamp\n"
      "s_waitcnt vmcnt(2)\n"
      "v_pk_sub_u16 v60, v60, v76 clamp\n"
      "v_pk_sub_u16 v61, v61, v76 clamp\n"
      "v_pk_sub_u16 v62, v62, v76 clamp\n"
      "v_pk_sub_u16 v63, v63, v76 clamp\n"
      "v_pk_sub_u16 v64, v64, v76 clamp\n"
      "v_pk_sub_u16 v65, v65, v76 clamp\n"
      "v_pk_sub_u16 v66, v66, v76 clamp\n"
      "v_pk_sub_u16 v67, v67, v76 clamp\n"
      "v_pk_sub_u16 v68, v68, v76 clamp\n"
      "v_pk_sub_u16 v69, v69, v76 clamp\n"
      "v_pk_sub_u16 v70, v70, v76 clamp\n"
      "v_pk_sub_u16 v71, v71, v76 clamp\n"
      "v_pk_sub_u16 v72, v72, v76 clamp\n"
      "v_pk_sub_u16 v73, v73, v76 clamp\n"
      "v_pk_sub_u16 v74, v74, v76 clamp\n"
      "v_pk_sub_u16 v75, v75, v76 clamp\n"
      "v_pk_lshlrev_b16 v60, 3, v60 op_sel_hi:[0,1]\n"
      "v_pk_lshlrev_b16 v61, 3, v61 op_sel_hi:[0,1]\n"
      "v_pk_lshlrev_b16 v62, 3, v62 op_sel_hi:[0,1]\n"
      "v_pk_lshlrev_b16 v63, 3, v63 op_sel_hi:[0,1]\n"
      "v_pk_lshlrev_b16 v64, 3, v64 op_sel_hi:[0,1]\n"
      "v_pk_lshlrev_b16 v65, 3, v65 op_sel_hi:[0,1]\n"
      "v_pk_lshlrev_b16 v66, 3, v66 op_sel_hi:[0,1]\n"
      "v_pk_lshlrev_b16 v67, 3, v67 op_sel_hi:[0,1]\n"
      "v_pk_lshlrev_b16 v68, 3, v68 op_sel_hi:[0,1]\n"
      "v_pk_lshlrev_b16 v69, 3, v69 op_sel_hi:[0,1]\n"
      "v_pk_lshlrev_b16 v70, 3, v70 op_sel_hi:[0,1]\n"
      "v_pk_lshlrev_b16 v71, 3, v71 op_sel_hi:[0,1]\n"
      "v_pk_lshlrev_b16 v72, 3, v72 op_sel_hi:[0,1]\n"
      "v_pk_lshlrev_b16 v73, 3, v73 op_sel_hi:[0,1]\n"
      "v_pk_lshlrev_b16 v74, 3, v74 op_sel_hi:[0,1]\n"
      "v_pk_lshlrev_b16 v75, 3, v75 op_sel_hi:[0,1]\n"
      "v_bfi_b32 v60, s77, v60, v24\n"
      "v_bfi_b32 v61, s77, v61, v25\n"
      "v_bfi_b32 v62, s77, v62, v26\n"
      "v_bfi_b32 v63, s77, v63, v27\n"
      "v_bfi_b32 v64, s77, v64, v28\n"
      "v_bfi_b32 v65, s77, v65, v29\n"
      "v_bfi_b32 v66, s77, v66, v30\n"
      "v_bfi_b32 v67, s77, v67, v31\n"
      "v_bfi_b32 v68, s77, v68, v32\n"
      "v_bfi_b32 v69, s77, v69, v33\n"
      "v_bfi_b32 v70, s77, v70, v34\n"
      "v_bfi_b32 v71, s77, v71, v35\n"
      "v_bfi_b32 v72, s77, v72, v36\n"
      "v_bfi_b32 v73, s77, v73, v37\n"
      "v_bfi_b32 v74, s77, v74, v38\n"
      "v_bfi_b32 v75, s77, v75, v39\n"
      "v_perm_b32 v80, v61, v60, s78\n"
      "v_perm_b32 v81, v63, v62, s78\n"
      "v_perm_b32 v82, v65, v64, s78\n"
      "v_perm_b32 v83, v67, v66, s78\n"
      "ds_write_b128 v7, v[80:83] offset:0\n"
      "v_perm_b32 v84, v61, v60, s79\n"
      "v_perm_b32 v85, v63, v62, s79\n"
      "v_perm_b32 v86, v65, v64, s79\n"
      "v_perm_b32 v87, v67, v66, s79\n"
      "ds_write_b128 v7, v[84:87] offset:16\n"
      "v_perm_b32 v88, v69, v68, s78\n"
      "v_perm_b32 v89, v71, v70, s78\n"
      "v_perm_b32 v90, v73, v72, s78\n"
      "v_perm_b32 v91, v75, v74, s78\n"
      "v_bfe_u32 v77, v76, 7, 8\n"
      "v_add_u32 v77, -112, v77\n"
      "v_cndmask_b32 v89, v89, v77, s[70:71]\n"
      "ds_write_b128 v7, v[88:91] offset:512\n"
      "v_perm_b32 v92, v69, v68, s79\n"
      "v_perm_b32 v93, v71, v70, s79\n"
      "v_perm_b32 v94, v73, v72, s79\n"
      "v_perm_b32 v95, v75, v74, s79\n"
      "v_bfe_u32 v77, v76, 23, 8\n"
      "v_add_u32 v77, -112, v77\n"
      "v_cndmask_b32 v93, v93, v77, s[70:71]\n"
      "ds_write_b128 v7, v[92:95] offset:528\n"
      "v_lshlrev_b32 v78, 16, v78\n"
      "v_readfirstlane_b32 s90, v56\n"
      "v_readfirstlane_b32 s91, v57\n"
      "s_nop 0\n"
      "s_add_u32 s90, s90, 0x9e3779b1\n"
      "s_add_u32 s91, s91, 0x85ebca77\n"
      "ds_write_b32 v6, v78 offset:256\n"
      "global_load_dwordx4 v[124:127], v8, s[94:95] offset:0 sc1\n"
      "global_load_dwordx4 v[128:131], v8, s[94:95] offset:128 sc1\n"
      "global_load_dwordx4 v[132:135], v8, s[94:95] offset:256 sc1\n"
      "global_load_dwordx4 v[136:139], v8, s[94:95] offset:384 sc1\n"
      "global_load_dwordx4 v[140:143], v8, s[94:95] offset:512 sc1\n"
      "global_load_dwordx4 v[144:147], v8, s[94:95] offset:640 sc1\n"
      "global_load_dwordx4 v[148:151], v8, s[94:95] offset:768 sc1\n"
      "global_load_dwordx4 v[152:155], v8, s[94:95] offset:896 sc1\n"
      "s_setprio 2\n"
      "ds_read_b128 v[16:19], v12 offset:0\n"
      "ds_read_b128 v[20:23], v12 offset:512\n"
      "ds_read_b128 v[24:27], v12 offset:1024\n"
      "ds_read_b64 v[28:29], v12 offset:1536\n"
      "ds_read_b128 v[32:35], v12 offset:2048\n"
      "ds_read_b128 v[36:39], v12 offset:2560\n"
      "ds_read_b128 v[40:43], v12 offset:3072\n"
      "ds_read_b64 v[44:45], v12 offset:3584\n"
      "v_mov_b32 v250, 0xff800000\n"
      "v_add_u32 v234, 64800, v13\n"
      "v_add_u32 v235, 129600, v13\n"
      "v_bfe_u32 v242, v14, 2, 5\n"
      "v_mov_b32 v246, 1\n"
      "v_and_b32 v251, 0xfffffe00, v14\n"
      "s_waitcnt lgkmcnt(7)\n"
      "v_alignbit_b32 v208, v17, v16, v242\n"
      "v_alignbit_b32 v209, v18, v17, v242\n"
      "v_alignbit_b32 v210, v19, v18, v242\n"
      "s_waitcnt lgkmcnt(6)\n"
      "v_alignbit_b32 v211, v20, v19, v242\n"
      "v_alignbit_b32 v212, v21, v20, v242\n"
      "v_alignbit_b32 v213, v22, v21, v242\n"
      "v_alignbit_b32 v214, v23, v22, v242\n"
      "s_waitcnt lgkmcnt(5)\n"
      "v_alignbit_b32 v215, v24, v23, v242\n"
      "v_alignbit_b32 v216, v25, v24, v242\n"
      "v_alignbit_b32 v217, v26, v25, v242\n"
      "v_alignbit_b32 v218, v27, v26, v242\n"
      "s_waitcnt lgkmcnt(4)\n"
      "v_alignbit_b32 v219, v28, v27, v242\n"
      "v_alignbit_b32 v220, v16, v28, v242\n"
      "s_waitcnt lgkmcnt(3)\n"
      "v_alignbit_b32 v221, v33, v32, v242\n"
      "v_alignbit_b32 v222, v34, v33, v242\n"
      "v_alignbit_b32 v223, v35, v34, v242\n"
      "s_waitcnt lgkmcnt(2)\n"
      "v_alignbit_b32 v224, v36, v35, v242\n"
      "v_alignbit_b32 v225, v37, v36, v242\n"
      "v_alignbit_b32 v226, v38, v37, v242\n"
      "v_alignbit_b32 v227, v39, v38, v242\n"
      "s_waitcnt lgkmcnt(1)\n"
      "v_alignbit_b32 v228, v40, v39, v242\n"
      "v_alignbit_b32 v229, v41, v40, v242\n"
      "v_alignbit_b32 v230, v42, v41, v242\n"
      "v_alignbit_b32 v231, v43, v42, v242\n"
      "s_waitcnt lgkmcnt(0)\n"
      "v_alignbit_b32 v232, v44, v43, v242\n"
      "v_alignbit_b32 v233, v32, v44, v242\n"
      "v_mul_f16 v156, v215, v208\n"
      "v_fma_mixhi_f16 v156, v216, v209, 0 op_sel_hi:[1,1,0]\n"
      "v_mul_f16 v157, v217, v210\n"
      "v_fma_mixhi_f16 v157, v218, v211, 0 op_sel_hi:[1,1,0]\n"
      "v_mul_f16 v158, v219, v212\n"
      "v_fma_mixhi_f16 v158, v220, v213, 0 op_sel_hi:[1,1,0]\n"
      "v_pk_mul_f16 v159, v208, v208 op_sel_hi:[0,1]\n"
      "v_mul_f16 v160, v228, v221\n"
      "v_fma_mixhi_f16 v160, v229, v222, 0 op_sel_hi:[1,1,0]\n"
      "v_mul_f16 v161, v230, v223\n"
      "v_fma_mixhi_f16 v161, v231, v224, 0 op_sel_hi:[1,1,0]\n"
      "v_mul_f16 v162, v232, v225\n"
      "v_fma_mixhi_f16 v162, v233, v226, 0 op_sel_hi:[1,1,0]\n"
      "v_pk_mul_f16 v163, v221, v221 op_sel_hi:[0,1]\n"
      "v_pk_mul_f16 v164, v208, v209 op_sel_hi:[0,1]\n"
      "v_pk_mul_f16 v165, v208, v210 op_sel_hi:[0,1]\n"
      "v_pk_mul_f16 v166, v208, v211 op_sel_hi:[0,1]\n"
      "v_pk_mul_f16 v167, v208, v212 op_sel_hi:[0,1]\n"
      "v_pk_mul_f16 v168, v221, v222 op_sel_hi:[0,1]\n"
      "v_pk_mul_f16 v169, v221, v223 op_sel_hi:[0,1]\n"
      "v_pk_mul_f16 v170, v221, v224 op_sel_hi:[0,1]\n"
      "v_pk_mul_f16 v171, v221, v225 op_sel_hi:[0,1]\n"
      "v_pk_mul_f16 v172, v208, v213 op_sel_hi:[0,1]\n"
      "v_pk_mul_f16 v173, v208, v214 op_sel_hi:[0,1]\n"
      "v_pk_mul_f16 v174, v209, v209 op_sel_hi:[0,1]\n"
      "v_pk_mul_f16 v175, v209, v210 op_sel_hi:[0,1]\n"
      "v_pk_mul_f16 v176, v221, v226 op_sel_hi:[0,1]\n"
      "v_pk_mul_f16 v177, v221, v227 op_sel_hi:[0,1]\n"
      "v_pk_mul_f16 v178, v222, v222 op_sel_hi:[0,1]\n"
      "v_pk_mul_f16 v179, v222, v223 op_sel_hi:[0,1]\n"
      "v_pk_mul_f16 v180, v209, v211 op_sel_hi:[0,1]\n"
      "v_pk_mul_f16 v181, v209, v212 op_sel_hi:[0,1]\n"
      "v_pk_mul_f16 v182, v209, v213 op_sel_hi:[0,1]\n"
      "v_pk_mul_f16 v183, v209, v214 op_sel_hi:[0,1]\n"
      "v_pk_mul_f16 v184, v222, v224 op_sel_hi:[0,1]\n"
      "v_pk_mul_f16 v185, v222, v225 op_sel_hi:[0,1]\n"
      "v_pk_mul_f16 v186, v222, v226 op_sel_hi:[0,1]\n"
      "v_pk_mul_f16 v187, v222, v227 op_sel_hi:[0,1]\n"
      "v_pk_mul_f16 v188, v209, v215 op_sel_hi:[0,1]\n"
      "v_pk_mul_f16 v189, v210, v210 op_sel_hi:[0,1]\n"
      "v_pk_mul_f16 v190, v210, v211 op_sel_hi:[0,1]\n"
      "v_pk_mul_f16 v191, v210, v212 op_sel_hi:[0,1]\n"
      "v_pk_mul_f16 v192, v222, v228 op_sel_hi:[0,1]\n"
      "v_pk_mul_f16 v193, v223, v223 op_sel_hi:[0,1]\n"
      "v_pk_mul_f16 v194, v223, v224 op_sel_hi:[0,1]\n"
      "v_pk_mul_f16 v195, v223, v225 op_sel_hi:[0,1]\n"
      "v_pk_mul_f16 v196, v210, v213 op_sel_hi:[0,1]\n"
      "v_pk_mul_f16 v197, v210, v214 op_sel_hi:[0,1]\n"
      "v_pk_mul_f16 v198, v210, v215 op_sel_hi:[0,1]\n"
      "v_pk_mul_f16 v199, v210, v216 op_sel_hi:[0,1]\n"
      "v_pk_mul_f16 v200, v223, v226 op_sel_hi:[0,1]\n"
      "v_pk_mul_f16 v201, v223, v227 op_sel_hi:[0,1]\n"
      "v_pk_mul_f16 v202, v223, v228 op_sel_hi:[0,1]\n"
      "v_pk_mul_f16 v203, v223, v229 op_sel_hi:[0,1]\n"
      "s_mov_b32 s88, 0x40000\n"
      "q_x_a:\n"
      "s_waitcnt vmcnt(7)\n"
      "v_bitop3_b32 v205, v124, v126, s90 bitop3:0x96\n"
      "v_bitop3_b32 v206, v125, v127, s91 bitop3:0x96\n"
      "ds_write_b64 v10, v[124:125] offset:1728\n"
      "v_or_b32 v204, v205, v206\n"
      "s_waitcnt vmcnt(6)\n"
      "v_bitop3_b32 v205, v128, v130, s90 bitop3:0x96\n"
      "v_bitop3_b32 v206, v129, v131, s91 bitop3:0x96\n"
      "ds_write_b64 v10, v[128:129] offset:5184\n"
      "v_or3_b32 v204, v204, v205, v206\n"
      "s_waitcnt vmcnt(5)\n"
      "v_bitop3_b32 v205, v132, v134, s90 bitop3:0x96\n"
      "v_bitop3_b32 v206, v133, v135, s91 bitop3:0x96\n"
      "ds_write_b64 v10, v[132:133] offset:8640\n"
      "v_or3_b32 v204, v204, v205, v206\n"
      "s_waitcnt vmcnt(4)\n"
      "v_bitop3_b32 v205, v136, v138, s90 bitop3:0x96\n"
      "v_bitop3_b32 v206, v137, v139, s91 bitop3:0x96\n"
      "ds_write_b64 v10, v[136:137] offset:12096\n"
      "v_or3_b32 v204, v204, v205, v206\n"
      "s_waitcnt vmcnt(3)\n"
      "v_bitop3_b32 v205, v140, v142, s90 bitop3:0x96\n"
      "v_bitop3_b32 v206, v141, v143, s91 bitop3:0x96\n"
      "ds_write_b64 v10, v[140:141] offset:15552\n"
      "v_or3_b32 v204, v204, v205, v206\n"
      "s_waitcnt vmcnt(2)\n"
      "v_bitop3_b32 v205, v144, v146, s90 bitop3:0x96\n"
      "v_bitop3_b32 v206, v145, v147, s91 bitop3:0x96\n"
      "ds_write_b64 v10, v[144:145] offset:19008\n"
      "v_or3_b32 v204, v204, v205, v206\n"
      "s_waitcnt vmcnt(1)\n"
      "v_bitop3_b32 v205, v148, v150, s90 bitop3:0x96\n"
      "v_bitop3_b32 v206, v149, v151, s91 bitop3:0x96\n"
      "ds_write_b64 v10, v[148:149] offset:22464\n"
      "v_or3_b32 v204, v204, v205, v206\n"
      "s_waitcnt vmcnt(0)\n"
      "v_bitop3_b32 v205, v152, v154, s90 bitop3:0x96\n"
      "v_bitop3_b32 v206, v153, v155, s91 bitop3:0x96\n"
      "ds_write_b64 v10, v[152:153] offset:25920\n"
      "v_or3_b32 v204, v204, v205, v206\n"
      "v_cmp_ne_u32 vcc, 0, v204\n"
      "s_cbranch_vccz q_x_b\n"
      "s_sleep 1\n"
      "s_waitcnt lgkmcnt(0)\n"
      "global_load_dwordx4 v[124:127], v8, s[94:95] offset:0 sc1\n"
      "global_load_dwordx4 v[128:131], v8, s[94:95] offset:128 sc1\n"
      "global_load_dwordx4 v[132:135], v8, s[94:95] offset:256 sc1\n"
      "global_load_dwordx4 v[136:139], v8, s[94:95] offset:384 sc1\n"
      "global_load_dwordx4 v[140:143], v8, s[94:95] offset:512 sc1\n"
      "global_load_dwordx4 v[144:147], v8, s[94:95] offset:640 sc1\n"
      "global_load_dwordx4 v[148:151], v8, s[94:95] offset:768 sc1\n"
      "global_load_dwordx4 v[152:155], v8, s[94:95] offset:896 sc1\n"
      "s_waitcnt vmcnt(0)\n"
      "s_sub_u32 s88, s88, 1\n"
      "s_cbranch_scc0 q_x_a\n"
      "q_x_b:\n"
      "s_mov_b64 exec, 1\n"
      "ds_add_u32 v251, v246 offset:132\n"
      "s_mov_b64 exec, -1\n"
      "s_mov_b32 s88, 0x400000\n"
      "7:\n"
      "ds_read_b32 v204, v251 offset:132\n"
      "s_sub_u32 s88, s88, 1\n"
      "s_cbranch_scc1 8f\n"
      "s_waitcnt lgkmcnt(0)\n"
      "v_cmp_gt_u32 vcc, s86, v204\n"
      "s_cbranch_vccnz 7b\n"
      "8:\n"
      "s_setprio 3\n"
      "ds_read_b128 v[144:147], v13 offset:1728\n"
      "ds_read_b128 v[148:151], v13 offset:3456\n"
      "s_waitcnt lgkmcnt(1)\n"
      "v_mfma_f32_32x32x16_f16 v[48:63], v[144:147], v[156:159], 0\n"
      "ds_read_b128 v[152:155], v13 offset:5184\n"
      "v_mfma_f32_32x32x16_f16 v[64:79], v[144:147], v[160:163], 0\n"
      "s_waitcnt lgkmcnt(1)\n"
      "v_mfma_f32_32x32x16_f16 v[48:63], v[148:151], v[164:167], v[48:63]\n"
      "ds_read_b128 v[144:147], v13 offset:6912\n"
      "v_mfma_f32_32x32x16_f16 v[64:79], v[148:151], v[168:171], v[64:79]\n"
      "s_waitcnt lgkmcnt(1)\n"
      "v_mfma_f32_32x32x16_f16 v[48:63], v[152:155], v[172:175], v[48:63]\n"
      "ds_read_b32 v206, v251 offset:140\n"
      "ds_read_b128 v[148:151], v13 offset:8640\n"
      "v_mfma_f32_32x32x16_f16 v[64:79], v[152:155], v[176:179], v[64:79]\n"
      "s_waitcnt lgkmcnt(2)\n"
      "v_mfma_f32_32x32x16_f16 v[48:63], v[144:147], v[180:183], v[48:63]\n"
      "ds_read_b128 v[152:155], v13 offset:10368\n"
      "v_mfma_f32_32x32x16_f16 v[64:79], v[144:147], v[184:187], v[64:79]\n"
      "s_waitcnt lgkmcnt(1)\n"
      "v_mfma_f32_32x32x16_f16 v[48:63], v[148:151], v[188:191], v[48:63]\n"
      "v_cmp_gt_u32 vcc, s86, v206\n"
      "s_cbranch_vccnz q_slow1\n"
      "q_back1:\n"
      "ds_read_b128 v[144:147], v13 offset:2592\n"
      "v_mfma_f32_32x32x16_f16 v[64:79], v[148:151], v[192:195], v[64:79]\n"
      "s_waitcnt lgkmcnt(1)\n"
      "v_mfma_f32_32x32x16_f16 v[48:63], v[152:155], v[196:199], v[48:63]\n"
      "ds_read_b128 v[148:151], v13 offset:4320\n"
      "v_mfma_f32_32x32x16_f16 v[64:79], v[152:155], v[200:203], v[64:79]\n"
      "s_waitcnt lgkmcnt(1)\n"
      "v_mfma_f32_32x32x16_f16 v[80:95], v[144:147], v[156:159], 0\n"
      "ds_read_b128 v[152:155], v13 offset:6048\n"
      "v_mfma_f32_32x32x16_f16 v[96:111], v[144:147], v[160:163], 0\n"
      "s_waitcnt lgkmcnt(1)\n"
      "v_mfma_f32_32x32x16_f16 v[80:95], v[148:151], v[164:167], v[80:95]\n"
      "ds_read_b128 v[144:147], v13 offset:7776\n"
      "v_mfma_f32_32x32x16_f16 v[96:111], v[148:151], v[168:171], v[96:111]\n"
      "s_waitcnt lgkmcnt(1)\n"
      "v_mfma_f32_32x32x16_f16 v[80:95], v[152:155], v[172:175], v[80:95]\n"
      "ds_read_b128 v[148:151], v13 offset:9504\n"
      "v_mfma_f32_32x32x16_f16 v[96:111], v[152:155], v[176:179], v[96:111]\n"
      "s_waitcnt lgkmcnt(1)\n"
      "v_mfma_f32_32x32x16_f16 v[80:95], v[144:147], v[180:183], v[80:95]\n"
      "ds_read_b128 v[152:155], v13 offset:11232\n"
      "v_mfma_f32_32x32x16_f16 v[96:111], v[144:147], v[184:187], v[96:111]\n"
      "s_waitcnt lgkmcnt(1)\n"
      "v_mfma_f32_32x32x16_f16 v[80:95], v[148:151], v[188:191], v[80:95]\n"
      "ds_read_b128 v[144:147], v13 offset:0\n"
      "v_pack_b32_f16 v156, v208, v209\n"
      "v_pack_b32_f16 v157, v210, v211\n"
      "v_pack_b32_f16 v158, v212, v213\n"
      "v_pack_b32_f16 v159, v214, v215\n"
      "v_mfma_f32_32x32x16_f16 v[96:111], v[148:151], v[192:195], v[96:111]\n"
      "v_pack_b32_f16 v160, v221, v222\n"
      "v_pack_b32_f16 v161, v223, v224\n"
      "v_pack_b32_f16 v162, v225, v226\n"
      "v_pack_b32_f16 v163, v227, v228\n"
      "s_waitcnt lgkmcnt(1)\n"
      "v_mfma_f32_32x32x16_f16 v[80:95], v[152:155], v[196:199], v[80:95]\n"
      "ds_read_b128 v[148:151], v13 offset:864\n"
      "v_pack_b32_f16 v164, v216, v217\n"
      "v_pack_b32_f16 v165, v218, v219\n"
      "v_mov_b32 v166, v220\n"
      "v_mov_b32 v167, 0\n"
      "v_mfma_f32_32x32x16_f16 v[96:111], v[152:155], v[200:203], v[96:111]\n"
      "v_pack_b32_f16 v168, v229, v230\n"
      "v_pack_b32_f16 v169, v231, v232\n"
      "v_mov_b32 v170, v233\n"
      "v_mov_b32 v171, 0\n"
      "s_waitcnt lgkmcnt(1)\n"
      "v_mfma_f32_32x32x16_f16 v[112:127], v[144:147], v[156:159], 0\n"
      "ds_read_b32 v204, v251 offset:404\n"
      "ds_read2_b64 v[172:175], v14 offset0:0 offset1:2\n"
      "ds_read2_b64 v[176:179], v14 offset0:4 offset1:6\n"
      "ds_read_b128 v[152:155], v13 offset:12096\n"
      "v_pk_mul_f16 v156, v211, v211 op_sel_hi:[0,1]\n"
      "v_pk_mul_f16 v157, v211, v212 op_sel_hi:[0,1]\n"
      "v_pk_mul_f16 v158, v211, v213 op_sel_hi:[0,1]\n"
      "v_pk_mul_f16 v159, v211, v214 op_sel_hi:[0,1]\n"
      "v_mfma_f32_32x32x16_f16 v[128:143], v[144:147], v[160:163], 0\n"
      "v_pk_mul_f16 v160, v224, v224 op_sel_hi:[0,1]\n"
      "v_pk_mul_f16 v161, v224, v225 op_sel_hi:[0,1]\n"
      "v_pk_mul_f16 v162, v224, v226 op_sel_hi:[0,1]\n"
      "v_pk_mul_f16 v163, v224, v227 op_sel_hi:[0,1]\n"
      "s_waitcnt lgkmcnt(4)\n"
      "v_mfma_f32_32x32x16_f16 v[112:127], v[148:151], v[164:167], v[112:127]\n"
      "ds_read_b128 v[144:147], v13 offset:12960\n"
      "v_mfma_f32_32x32x16_f16 v[128:143], v[148:151], v[168:171], v[128:143]\n"
      "s_waitcnt lgkmcnt(1)\n"
      "v_mfma_f32_32x32x16_f16 v[48:63], v[152:155], v[156:159], v[48:63]\n"
      "ds_read_b128 v[148:151], v13 offset:13824\n"
      "v_pk_mul_f16 v164, v211, v215 op_sel_hi:[0,1]\n"
      "v_pk_mul_f16 v165, v211, v216 op_sel_hi:[0,1]\n"
      "v_pk_mul_f16 v166, v211, v217 op_sel_hi:[0,1]\n"
      "v_pk_mul_f16 v167, v212, v212 op_sel_hi:[0,1]\n"
      "v_mfma_f32_32x32x16_f16 v[64:79], v[152:155], v[160:163], v[64:79]\n"
      "v_pk_mul_f16 v168, v224, v228 op_sel_hi:[0,1]\n"
      "v_pk_mul_f16 v169, v224, v229 op_sel_hi:[0,1]\n"
      "v_pk_mul_f16 v170, v224, v230 op_sel_hi:[0,1]\n"
      "v_pk_mul_f16 v171, v225, v225 op_sel_hi:[0,1]\n"
      "s_waitcnt lgkmcnt(1)\n"
      "v_mfma_f32_32x32x16_f16 v[80:95], v[144:147], v[156:159], v[80:95]\n"
      "ds_read_b32 v205, v251 offset:136\n"
      "ds_read_b128 v[152:155], v13 offset:14688\n"
      "v_mfma_f32_32x32x16_f16 v[96:111], v[144:147], v[160:163], v[96:111]\n"
      "v_readfirstlane_b32 s82, v204\n"
      "ds_read_b128 v[188:191], v14 offset:256\n"
      "ds_read_b128 v[192:195], v14 offset:272\n"
      "ds_read_b128 v[196:199], v14 offset:288\n"
      "ds_read_b128 v[200:203], v14 offset:304\n"
      "s_waitcnt lgkmcnt(6)\n"
      "v_mfma_f32_32x32x16_f16 v[48:63], v[148:151], v[164:167], v[48:63]\n"
      "ds_read_b128 v[144:147], v13 offset:15552\n"
      "v_lshlrev_b32 v236, 1, v29\n"
      "v_lshlrev_b32 v237, 1, v45\n"
      "v_add_u32 v242, v173, v29\n"
      "v_add_u32 v246, v173, v45\n"
      "v_add_u32 v30, v172, v236\n"
      "v_mfma_f32_32x32x16_f16 v[64:79], v[148:151], v[168:171], v[64:79]\n"
      "v_add_u32 v46, v172, v237\n"
      "v_ldexp_f32 v242, 1.0, v242\n"
      "v_ldexp_f32 v246, 1.0, v246\n"
      "v_ldexp_f32 v30, 1.0, v30\n"
      "v_ldexp_f32 v46, 1.0, v46\n"
      "s_waitcnt lgkmcnt(5)\n"
      "v_mfma_f32_32x32x16_f16 v[80:95], v[152:155], v[164:167], v[80:95]\n"
      "v_cmp_gt_u32 vcc, 4, v205\n"
      "s_cbranch_vccnz q_slow\n"
      "q_back:\n"
      "ds_read_b128 v[148:151], v13 offset:16416\n"
      "v_pk_mul_f16 v156, v212, v213 op_sel_hi:[0,1]\n"
      "v_pk_mul_f16 v157, v212, v214 op_sel_hi:[0,1]\n"
      "v_pk_mul_f16 v158, v212, v215 op_sel_hi:[0,1]\n"
      "v_pk_mul_f16 v159, v212, v216 op_sel_hi:[0,1]\n"
      "v_add_u32 v243, v175, v29\n"
      "v_mfma_f32_32x32x16_f16 v[96:111], v[152:155], v[168:171], v[96:111]\n"
      "v_pk_mul_f16 v160, v225, v226 op_sel_hi:[0,1]\n"
      "v_pk_mul_f16 v161, v225, v227 op_sel_hi:[0,1]\n"
      "v_pk_mul_f16 v162, v225, v228 op_sel_hi:[0,1]\n"
      "v_pk_mul_f16 v163, v225, v229 op_sel_hi:[0,1]\n"
      "v_add_u32 v247, v175, v45\n"
      "s_waitcnt lgkmcnt(1)\n"
      "v_mfma_f32_32x32x16_f16 v[48:63], v[144:147], v[156:159], v[48:63]\n"
      "ds_read_b128 v[152:155], v13 offset:17280\n"
      "v_add_u32 v31, v174, v236\n"
      "v_add_u32 v47, v174, v237\n"
      "v_ldexp_f32 v243, 1.0, v243\n"
      "v_ldexp_f32 v247, 1.0, v247\n"
      "v_ldexp_f32 v31, 1.0, v31\n"
      "v_mfma_f32_32x32x16_f16 v[64:79], v[144:147], v[160:163], v[64:79]\n"
      "v_ldexp_f32 v47, 1.0, v47\n"
      "v_add_u32 v244, v177, v29\n"
      "v_add_u32 v248, v177, v45\n"
      "v_add_u32 v125, v176, v236\n"
      "v_add_u32 v141, v176, v237\n"
      "s_waitcnt lgkmcnt(1)\n"
      "v_mfma_f32_32x32x16_f16 v[80:95], v[148:151], v[156:159], v[80:95]\n"
      "ds_read_b128 v[144:147], v13 offset:18144\n"
      "v_pk_mul_f16 v156, v212, v217 op_sel_hi:[0,1]\n"
      "v_pk_mul_f16 v157, v212, v218 op_sel_hi:[0,1]\n"
      "v_pk_mul_f16 v158, v213, v213 op_sel_hi:[0,1]\n"
      "v_pk_mul_f16 v159, v213, v214 op_sel_hi:[0,1]\n"
      "v_ldexp_f32 v244, 1.0, v244\n"
      "v_mfma_f32_32x32x16_f16 v[96:111], v[148:151], v[160:163], v[96:111]\n"
      "v_pk_mul_f16 v160, v225, v230 op_sel_hi:[0,1]\n"
      "v_pk_mul_f16 v161, v225, v231 op_sel_hi:[0,1]\n"
      "v_pk_mul_f16 v162, v226, v226 op_sel_hi:[0,1]\n"
      "v_pk_mul_f16 v163, v226, v227 op_sel_hi:[0,1]\n"
      "v_ldexp_f32 v248, 1.0, v248\n"
      "s_waitcnt lgkmcnt(1)\n"
      "v_mfma_f32_32x32x16_f16 v[48:63], v[152:155], v[156:159], v[48:63]\n"
      "ds_read_b128 v[148:151], v13 offset:19008\n"
      "v_ldexp_f32 v125, 1.0, v125\n"
      "v_ldexp_f32 v141, 1.0, v141\n"
      "v_add_u32 v245, v179, v29\n"
      "v_add_u32 v249, v179, v45\n"
      "v_add_u32 v126, v178, v236\n"
      "v_mfma_f32_32x32x16_f16 v[64:79], v[152:155], v[160:163], v[64:79]\n"
      "v_add_u32 v142, v178, v237\n"
      "v_ldexp_f32 v245, 1.0, v245\n"
      "v_ldexp_f32 v249, 1.0, v249\n"
      "v_ldexp_f32 v126, 1.0, v126\n"
      "v_ldexp_f32 v142, 1.0, v142\n"
      "s_waitcnt lgkmcnt(1)\n"
      "v_mfma_f32_32x32x16_f16 v[80:95], v[144:147], v[156:159], v[80:95]\n"
      "ds_read_b128 v[152:155], v13 offset:19872\n"
      "v_pk_mul_f16 v156, v213, v215 op_sel_hi:[0,1]\n"
      "v_pk_mul_f16 v157, v213, v216 op_sel_hi:[0,1]\n"
      "v_pk_mul_f16 v158, v213, v217 op_sel_hi:[0,1]\n"
      "v_pk_mul_f16 v159, v213, v218 op_sel_hi:[0,1]\n"
      "v_fma_f32 v112, v112, v242, v188\n"
      "v_mfma_f32_32x32x16_f16 v[96:111], v[144:147], v[160:163], v[96:111]\n"
      "v_pk_mul_f16 v160, v226, v228 op_sel_hi:[0,1]\n"
      "v_pk_mul_f16 v161, v226, v229 op_sel_hi:[0,1]\n"
      "v_pk_mul_f16 v162, v226, v230 op_sel_hi:[0,1]\n"
      "v_pk_mul_f16 v163, v226, v231 op_sel_hi:[0,1]\n"
      "v_fma_f32 v128, v128, v246, v188\n"
      "s_waitcnt lgkmcnt(1)\n"
      "v_mfma_f32_32x32x16_f16 v[48:63], v[148:151], v[156:159], v[48:63]\n"
      "ds_read_b128 v[144:147], v13 offset:20736\n"
      "v_fma_f32 v113, v113, v242, v189\n"
      "v_fma_f32 v129, v129, v246, v189\n"
      "v_fma_f32 v114, v114, v242, v190\n"
      "v_fma_f32 v130, v130, v246, v190\n"
      "v_fma_f32 v115, v115, v242, v191\n"
      "v_mfma_f32_32x32x16_f16 v[64:79], v[148:151], v[160:163], v[64:79]\n"
      "v_fma_f32 v131, v131, v246, v191\n"
      "v_fma_f32 v116, v116, v243, v192\n"
      "v_fma_f32 v132, v132, v247, v192\n"
      "v_fma_f32 v117, v117, v243, v193\n"
      "v_fma_f32 v133, v133, v247, v193\n"
      "s_waitcnt lgkmcnt(1)\n"
      "v_mfma_f32_32x32x16_f16 v[80:95], v[152:155], v[156:159], v[80:95]\n"
      "ds_read_b128 v[148:151], v13 offset:21600\n"
      "v_pk_mul_f16 v156, v213, v219 op_sel_hi:[0,1]\n"
      "v_pk_mul_f16 v157, v214, v214 op_sel_hi:[0,1]\n"
      "v_pk_mul_f16 v158, v214, v215 op_sel_hi:[0,1]\n"
      "v_pk_mul_f16 v159, v214, v216 op_sel_hi:[0,1]\n"
      "v_fma_f32 v118, v118, v243, v194\n"
      "v_mfma_f32_32x32x16_f16 v[96:111], v[152:155], v[160:163], v[96:111]\n"
      "v_pk_mul_f16 v160, v226, v232 op_sel_hi:[0,1]\n"
      "v_pk_mul_f16 v161, v227, v227 op_sel_hi:[0,1]\n"
      "v_pk_mul_f16 v162, v227, v228 op_sel_hi:[0,1]\n"
      "v_pk_mul_f16 v163, v227, v229 op_sel_hi:[0,1]\n"
      "v_fma_f32 v134, v134, v247, v194\n"
      "s_waitcnt lgkmcnt(1)\n"
      "v_mfma_f32_32x32x16_f16 v[48:63], v[144:147], v[156:159], v[48:63]\n"
      "ds_read_b128 v[152:155], v13 offset:22464\n"
      "v_fma_f32 v119, v119, v243, v195\n"
      "v_fma_f32 v135, v135, v247, v195\n"
      "v_fma_f32 v120, v120, v244, v196\n"
      "v_fma_f32 v136, v136, v248, v196\n"
      "v_fma_f32 v121, v121, v244, v197\n"
      "v_mfma_f32_32x32x16_f16 v[64:79], v[144:147], v[160:163], v[64:79]\n"
      "v_fma_f32 v137, v137, v248, v197\n"
      "v_fma_f32 v122, v122, v244, v198\n"
      "v_fma_f32 v138, v138, v248, v198\n"
      "v_fma_f32 v123, v123, v244, v199\n"
      "v_fma_f32 v139, v139, v248, v199\n"
      "s_waitcnt lgkmcnt(1)\n"
      "v_mfma_f32_32x32x16_f16 v[80:95], v[148:151], v[156:159], v[80:95]\n"
      "ds_read_b128 v[144:147], v13 offset:23328\n"
      "v_pk_mul_f16 v156, v214, v217 op_sel_hi:[0,1]\n"
      "v_pk_mul_f16 v157, v214, v218 op_sel_hi:[0,1]\n"
      "v_pk_mul_f16 v158, v214, v219 op_sel_hi:[0,1]\n"
      "v_pk_mul_f16 v159, v214, v220 op_sel_hi:[0,1]\n"
      "v_fma_f32 v124, v124, v245, v200\n"
      "v_mfma_f32_32x32x16_f16 v[96:111], v[148:151], v[160:163], v[96:111]\n"
      "v_pk_mul_f16 v160, v227, v230 op_sel_hi:[0,1]\n"
      "v_pk_mul_f16 v161, v227, v231 op_sel_hi:[0,1]\n"
      "v_pk_mul_f16 v162, v227, v232 op_sel_hi:[0,1]\n"
      "v_pk_mul_f16 v163, v227, v233 op_sel_hi:[0,1]\n"
      "v_fma_f32 v140, v140, v249, v200\n"
      "s_waitcnt lgkmcnt(1)\n"
      "v_mfma_f32_32x32x16_f16 v[48:63], v[152:155], v[156:159], v[48:63]\n"
      "ds_read_b128 v[148:151], v13 offset:24192\n"
      "v_pk_mul_f16 v164, v215, v215 op_sel_hi:[0,1]\n"
      "v_pk_mul_f16 v165, v215, v216 op_sel_hi:[0,1]\n"
      "v_pk_mul_f16 v166, v215, v217 op_sel_hi:[0,1]\n"
      "v_pk_mul_f16 v167, v215, v218 op_sel_hi:[0,1]\n"
      "v_mfma_f32_32x32x16_f16 v[64:79], v[152:155], v[160:163], v[64:79]\n"
      "v_pk_mul_f16 v168, v228, v228 op_sel_hi:[0,1]\n"
      "v_pk_mul_f16 v169, v228, v229 op_sel_hi:[0,1]\n"
      "v_pk_mul_f16 v170, v228, v230 op_sel_hi:[0,1]\n"
      "v_pk_mul_f16 v171, v228, v231 op_sel_hi:[0,1]\n"
      "s_waitcnt lgkmcnt(1)\n"
      "v_mfma_f32_32x32x16_f16 v[80:95], v[144:147], v[156:159], v[80:95]\n"
      "ds_read_b128 v[152:155], v13 offset:25056\n"
      "v_mfma_f32_32x32x16_f16 v[96:111], v[144:147], v[160:163], v[96:111]\n"
      "s_waitcnt lgkmcnt(1)\n"
      "v_mfma_f32_32x32x16_f16 v[48:63], v[148:151], v[164:167], v[48:63]\n"
      "ds_read_b128 v[144:147], v13 offset:25920\n"
      "v_pk_mul_f16 v156, v215, v219 op_sel_hi:[0,1]\n"
      "v_pk_mul_f16 v157, v215, v220 op_sel_hi:[0,1]\n"
      "v_pk_mul_f16 v158, v216, v216 op_sel_hi:[0,1]\n"
      "v_pk_mul_f16 v159, v216, v217 op_sel_hi:[0,1]\n"
      "v_mfma_f32_32x32x16_f16 v[64:79], v[148:151], v[168:171], v[64:79]\n"
      "v_pk_mul_f16 v160, v228, v232 op_sel_hi:[0,1]\n"
      "v_pk_mul_f16 v161, v228, v233 op_sel_hi:[0,1]\n"
      "v_pk_mul_f16 v162, v229, v229 op_sel_hi:[0,1]\n"
      "v_pk_mul_f16 v163, v229, v230 op_sel_hi:[0,1]\n"
      "s_waitcnt lgkmcnt(1)\n"
      "v_mfma_f32_32x32x16_f16 v[80:95], v[152:155], v[164:167], v[80:95]\n"
      "ds_read_b128 v[148:151], v13 offset:26784\n"
      "v_mfma_f32_32x32x16_f16 v[96:111], v[152:155], v[168:171], v[96:111]\n"
      "s_waitcnt lgkmcnt(1)\n"
      "v_mfma_f32_32x32x16_f16 v[48:63], v[144:147], v[156:159], v[48:63]\n"
      "ds_read_b128 v[152:155], v13 offset:27648\n"
      "v_pk_mul_f16 v164, v216, v218 op_sel_hi:[0,1]\n"
      "v_pk_mul_f16 v165, v216, v219 op_sel_hi:[0,1]\n"
      "v_pk_mul_f16 v166, v216, v220 op_sel_hi:[0,1]\n"
      "v_pk_mul_f16 v167, v216, v208 op_sel_hi:[0,1]\n"
      "v_mfma_f32_32x32x16_f16 v[64:79], v[144:147], v[160:163], v[64:79]\n"
      "v_pk_mul_f16 v168, v229, v231 op_sel_hi:[0,1]\n"
      "v_pk_mul_f16 v169, v229, v232 op_sel_hi:[0,1]\n"
      "v_pk_mul_f16 v170, v229, v233 op_sel_hi:[0,1]\n"
      "v_pk_mul_f16 v171, v229, v221 op_sel_hi:[0,1]\n"
      "s_waitcnt lgkmcnt(1)\n"
      "v_mfma_f32_32x32x16_f16 v[80:95], v[148:151], v[156:159], v[80:95]\n"
      "ds_read_b128 v[144:147], v13 offset:28512\n"
      "v_mfma_f32_32x32x16_f16 v[96:111], v[148:151], v[160:163], v[96:111]\n"
      "s_waitcnt lgkmcnt(1)\n"
      "v_mfma_f32_32x32x16_f16 v[48:63], v[152:155], v[164:167], v[48:63]\n"
      "ds_read_b128 v[148:151], v13 offset:29376\n"
      "v_pk_mul_f16 v200, v217, v217 op_sel_hi:[0,1]\n"
      "v_pk_mul_f16 v201, v217, v218 op_sel_hi:[0,1]\n"
      "v_pk_mul_f16 v202, v217, v219 op_sel_hi:[0,1]\n"
      "v_pk_mul_f16 v203, v217, v220 op_sel_hi:[0,1]\n"
      "v_mfma_f32_32x32x16_f16 v[64:79], v[152:155], v[168:171], v[64:79]\n"
      "v_pk_mul_f16 v196, v230, v230 op_sel_hi:[0,1]\n"
      "v_pk_mul_f16 v197, v230, v231 op_sel_hi:[0,1]\n"
      "v_pk_mul_f16 v198, v230, v232 op_sel_hi:[0,1]\n"
      "v_pk_mul_f16 v199, v230, v233 op_sel_hi:[0,1]\n"
      "s_waitcnt lgkmcnt(1)\n"
      "v_mfma_f32_32x32x16_f16 v[80:95], v[144:147], v[164:167], v[80:95]\n"
      "ds_read_b128 v[152:155], v13 offset:30240\n"
      "v_pk_mul_f16 v192, v217, v208 op_sel_hi:[0,1]\n"
      "v_pk_mul_f16 v193, v217, v209 op_sel_hi:[0,1]\n"
      "v_pk_mul_f16 v194, v218, v218 op_sel_hi:[0,1]\n"
      "v_pk_mul_f16 v195, v218, v219 op_sel_hi:[0,1]\n"
      "v_mfma_f32_32x32x16_f16 v[96:111], v[144:147], v[168:171], v[96:111]\n"
      "v_pk_mul_f16 v188, v230, v221 op_sel_hi:[0,1]\n"
      "v_pk_mul_f16 v189, v230, v222 op_sel_hi:[0,1]\n"
      "v_pk_mul_f16 v190, v231, v231 op_sel_hi:[0,1]\n"
      "v_pk_mul_f16 v191, v231, v232 op_sel_hi:[0,1]\n"
      "s_waitcnt lgkmcnt(1)\n"
      "v_mfma_f32_32x32x16_f16 v[48:63], v[148:151], v[200:203], v[48:63]\n"
      "ds_read_b128 v[144:147], v13 offset:31104\n"
      "v_pk_mul_f16 v184, v218, v220 op_sel_hi:[0,1]\n"
      "v_pk_mul_f16 v185, v218, v208 op_sel_hi:[0,1]\n"
      "v_pk_mul_f16 v186, v218, v209 op_sel_hi:[0,1]\n"
      "v_pk_mul_f16 v187, v218, v210 op_sel_hi:[0,1]\n"
      "v_mfma_f32_32x32x16_f16 v[64:79], v[148:151], v[196:199], v[64:79]\n"
      "v_pk_mul_f16 v180, v231, v233 op_sel_hi:[0,1]\n"
      "v_pk_mul_f16 v181, v231, v221 op_sel_hi:[0,1]\n"
      "v_pk_mul_f16 v182, v231, v222 op_sel_hi:[0,1]\n"
      "v_pk_mul_f16 v183, v231, v223 op_sel_hi:[0,1]\n"
      "s_waitcnt lgkmcnt(1)\n"
      "v_mfma_f32_32x32x16_f16 v[48:63], v[152:155], v[192:195], v[48:63]\n"
      "ds_read_b128 v[148:151], v13 offset:31968\n"
      "v_pk_mul_f16 v176, v219, v219 op_sel_hi:[0,1]\n"
      "v_pk_mul_f16 v177, v219, v220 op_sel_hi:[0,1]\n"
      "v_pk_mul_f16 v178, v219, v208 op_sel_hi:[0,1]\n"
      "v_pk_mul_f16 v179, v219, v209 op_sel_hi:[0,1]\n"
      "v_mfma_f32_32x32x16_f16 v[64:79], v[152:155], v[188:191], v[64:79]\n"
      "v_pk_mul_f16 v172, v232, v232 op_sel_hi:[0,1]\n"
      "v_pk_mul_f16 v173, v232, v233 op_sel_hi:[0,1]\n"
      "v_pk_mul_f16 v174, v232, v221 op_sel_hi:[0,1]\n"
      "v_pk_mul_f16 v175, v232, v222 op_sel_hi:[0,1]\n"
      "s_waitcnt lgkmcnt(1)\n"
      "v_mfma_f32_32x32x16_f16 v[48:63], v[144:147], v[184:187], v[48:63]\n"
      "ds_read_b128 v[152:155], v13 offset:32832\n"
      "v_pk_mul_f16 v168, v219, v210 op_sel_hi:[0,1]\n"
      "v_pk_mul_f16 v169, v219, v211 op_sel_hi:[0,1]\n"
      "v_pk_mul_f16 v170, v220, v220 op_sel_hi:[0,1]\n"
      "v_pk_mul_f16 v171, v220, v208 op_sel_hi:[0,1]\n"
      "v_mfma_f32_32x32x16_f16 v[64:79], v[144:147], v[180:183], v[64:79]\n"
      "v_pk_mul_f16 v164, v232, v223 op_sel_hi:[0,1]\n"
      "v_pk_mul_f16 v165, v232, v224 op_sel_hi:[0,1]\n"
      "v_pk_mul_f16 v166, v233, v233 op_sel_hi:[0,1]\n"
      "v_pk_mul_f16 v167, v233, v221 op_sel_hi:[0,1]\n"
      "s_waitcnt lgkmcnt(1)\n"
      "v_mfma_f32_32x32x16_f16 v[48:63], v[148:151], v[176:179], v[48:63]\n"
      "ds_read_b128 v[144:147], v13 offset:33696\n"
      "v_pk_mul_f16 v160, v220, v209 op_sel_hi:[0,1]\n"
      "v_pk_mul_f16 v161, v220, v210 op_sel_hi:[0,1]\n"
      "v_pk_mul_f16 v162, v220, v211 op_sel_hi:[0,1]\n"
      "v_pk_mul_f16 v163, v220, v212 op_sel_hi:[0,1]\n"
      "v_mfma_f32_32x32x16_f16 v[64:79], v[148:151], v[172:175], v[64:79]\n"
      "v_pk_mul_f16 v156, v233, v222 op_sel_hi:[0,1]\n"
      "v_pk_mul_f16 v157, v233, v223 op_sel_hi:[0,1]\n"
      "v_pk_mul_f16 v158, v233, v224 op_sel_hi:[0,1]\n"
      "v_pk_mul_f16 v159, v233, v225 op_sel_hi:[0,1]\n"
      "s_waitcnt lgkmcnt(1)\n"
      "v_mfma_f32_32x32x16_f16 v[48:63], v[152:155], v[168:171], v[48:63]\n"
      "ds_read_b128 v[148:151], v13 offset:34560\n"
      "v_mfma_f32_32x32x16_f16 v[64:79], v[152:155], v[164:167], v[64:79]\n"
      "s_waitcnt lgkmcnt(1)\n"
      "v_mfma_f32_32x32x16_f16 v[48:63], v[144:147], v[160:163], v[48:63]\n"
      "ds_read_b128 v[152:155], v13 offset:35424\n"
      "v_mfma_f32_32x32x16_f16 v[64:79], v[144:147], v[156:159], v[64:79]\n"
      "s_waitcnt lgkmcnt(1)\n"
      "v_mfma_f32_32x32x16_f16 v[80:95], v[148:151], v[200:203], v[80:95]\n"
      "ds_read_b128 v[144:147], v13 offset:36288\n"
      "v_max3_f32 v242, |v48|, |v49|, |v50|\n"
      "s_nop 1\n"
      "v_max3_f32 v246, |v64|, |v65|, |v66|\n"
      "v_max3_f32 v242, v242, |v51|, |v52|\n"
      "v_max3_f32 v246, v246, |v67|, |v68|\n"
      "v_max3_f32 v242, v242, |v53|, |v54|\n"
      "v_mfma_f32_32x32x16_f16 v[96:111], v[148:151], v[196:199], v[96:111]\n"
      "v_max3_f32 v246, v246, |v69|, |v70|\n"
      "v_max3_f32 v242, v242, |v55|, |v56|\n"
      "v_max3_f32 v246, v246, |v71|, |v72|\n"
      "v_max3_f32 v242, v242, |v57|, |v58|\n"
      "v_max3_f32 v246, v246, |v73|, |v74|\n"
      "s_waitcnt lgkmcnt(1)\n"
      "v_mfma_f32_32x32x16_f16 v[80:95], v[152:155], v[192:195], v[80:95]\n"
      "ds_read_b128 v[148:151], v13 offset:37152\n"
      "v_max3_f32 v242, v242, |v59|, |v60|\n"
      "v_max3_f32 v246, v246, |v75|, |v76|\n"
      "v_mov_b32 v243, v242\n"
      "v_mov_b32 v247, v246\n"
      "v_add_u32 v245, s82, v236\n"
      "v_mfma_f32_32x32x16_f16 v[96:111], v[152:155], v[188:191], v[96:111]\n"
      "v_add_u32 v249, s82, v237\n"
      "v_ldexp_f32 v240, v61, v245\n"
      "v_ldexp_f32 v241, v77, v249\n"
      "v_permlane32_swap_b32 v242, v243\n"
      "v_permlane32_swap_b32 v246, v247\n"
      "s_waitcnt lgkmcnt(1)\n"
      "v_mfma_f32_32x32x16_f16 v[80:95], v[144:147], v[184:187], v[80:95]\n"
      "ds_read_b128 v[152:155], v13 offset:38016\n"
      "v_max3_f32 v242, v242, v243, s81\n"
      "v_max3_f32 v246, v246, v247, s81\n"
      "v_lshrrev_b32 v242, 23, v242\n"
      "v_lshrrev_b32 v246, 23, v246\n"
      "v_mad_i32_i24 v244, v242, v250, s80\n"
      "v_mfma_f32_32x32x16_f16 v[96:111], v[144:147], v[180:183], v[96:111]\n"
      "v_mad_i32_i24 v248, v246, v250, s80\n"
      "v_fma_mixlo_f16 v208, v48, v244, 0\n"
      "v_fma_mixlo_f16 v221, v64, v248, 0\n"
      "v_fma_mixlo_f16 v209, v49, v244, 0\n"
      "v_fma_mixlo_f16 v222, v65, v248, 0\n"
      "s_waitcnt lgkmcnt(1)\n"
      "v_mfma_f32_32x32x16_f16 v[80:95], v[148:151], v[176:179], v[80:95]\n"
      "ds_read_b128 v[144:147], v13 offset:38880\n"
      "v_fma_mixlo_f16 v210, v50, v244, 0\n"
      "v_fma_mixlo_f16 v223, v66, v248, 0\n"
      "v_fma_mixlo_f16 v211, v51, v244, 0\n"
      "v_fma_mixlo_f16 v224, v67, v248, 0\n"
      "v_fma_mixlo_f16 v212, v52, v244, 0\n"
      "v_mfma_f32_32x32x16_f16 v[96:111], v[148:151], v[172:175], v[96:111]\n"
      "v_fma_mixlo_f16 v225, v68, v248, 0\n"
      "v_fma_mixlo_f16 v213, v53, v244, 0\n"
      "v_fma_mixlo_f16 v226, v69, v248, 0\n"
      "v_fma_mixlo_f16 v214, v54, v244, 0\n"
      "v_fma_mixlo_f16 v227, v70, v248, 0\n"
      "s_waitcnt lgkmcnt(1)\n"
      "v_mfma_f32_32x32x16_f16 v[80:95], v[152:155], v[168:171], v[80:95]\n"
      "s_barrier\n"
      "ds_read_b128 v[148:151], v13 offset:39744\n"
      "v_fma_mixlo_f16 v215, v55, v244, 0\n"
      "v_fma_mixlo_f16 v228, v71, v248, 0\n"
      "v_fma_mixlo_f16 v216, v56, v244, 0\n"
      "v_fma_mixlo_f16 v229, v72, v248, 0\n"
      "v_fma_mixlo_f16 v217, v57, v244, 0\n"
      "v_mfma_f32_32x32x16_f16 v[96:111], v[152:155], v[164:167], v[96:111]\n"
      "v_fma_mixlo_f16 v230, v73, v248, 0\n"
      "v_fma_mixlo_f16 v218, v58, v244, 0\n"
      "v_fma_mixlo_f16 v231, v74, v248, 0\n"
      "v_fma_mixlo_f16 v219, v59, v244, 0\n"
      "v_fma_mixlo_f16 v232, v75, v248, 0\n"
      "s_waitcnt lgkmcnt(1)\n"
      "v_mfma_f32_32x32x16_f16 v[80:95], v[144:147], v[160:163], v[80:95]\n"
      "ds_read_b128 v[152:155], v13 offset:40608\n"
      "v_fma_mixlo_f16 v220, v60, v244, 0\n"
      "v_fma_mixlo_f16 v233, v76, v248, 0\n"
      "v_add3_u32 v236, v236, v242, v29\n"
      "v_add3_u32 v237, v237, v246, v45\n"
      "v_mfma_f32_32x32x16_f16 v[96:111], v[144:147], v[156:159], v[96:111]\n"
      "v_pk_mul_f16 v156, v208, v16 op_sel_hi:[0,1]\n"
      "v_pk_mul_f16 v157, v208, v17 op_sel_hi:[0,1]\n"
      "v_pk_mul_f16 v158, v208, v18 op_sel_hi:[0,1]\n"
      "v_pk_mul_f16 v159, v208, v19 op_sel_hi:[0,1]\n"
      "v_pk_mul_f16 v160, v221, v32 op_sel_hi:[0,1]\n"
      "s_waitcnt lgkmcnt(1)\n"
      "v_mfma_f32_32x32x16_f16 v[48:63], v[148:151], v[156:159], 0\n"
      "ds_read_b128 v[144:147], v13 offset:41472\n"
      "v_pk_mul_f16 v161, v221, v33 op_sel_hi:[0,1]\n"
      "v_pk_mul_f16 v162, v221, v34 op_sel_hi:[0,1]\n"
      "v_pk_mul_f16 v163, v221, v35 op_sel_hi:[0,1]\n"
      "v_pk_mul_f16 v156, v208, v20 op_sel_hi:[0,1]\n"
      "v_pk_mul_f16 v157, v208, v21 op_sel_hi:[0,1]\n"
      "v_mfma_f32_32x32x16_f16 v[64:79], v[148:151], v[160:163], 0\n"
      "v_pk_mul_f16 v158, v208, v22 op_sel_hi:[0,1]\n"
      "v_pk_mul_f16 v159, v208, v23 op_sel_hi:[0,1]\n"
      "v_pk_mul_f16 v160, v221, v36 op_sel_hi:[0,1]\n"
      "v_pk_mul_f16 v161, v221, v37 op_sel_hi:[0,1]\n"
      "v_pk_mul_f16 v162, v221, v38 op_sel_hi:[0,1]\n"
      "s_waitcnt lgkmcnt(1)\n"
      "v_mfma_f32_32x32x16_f16 v[48:63], v[152:155], v[156:159], v[48:63]\n"
      "ds_read_b128 v[148:151], v13 offset:42336\n"
      "v_pk_mul_f16 v163, v221, v39 op_sel_hi:[0,1]\n"
      "v_pk_mul_f16 v156, v208, v24 op_sel_hi:[0,1]\n"
      "v_pk_mul_f16 v157, v208, v25 op_sel_hi:[0,1]\n"
      "v_pk_mul_f16 v158, v208, v26 op_sel_hi:[0,1]\n"
      "v_pk_mul_f16 v159, v208, v27 op_sel_hi:[0,1]\n"
      "v_mfma_f32_32x32x16_f16 v[64:79], v[152:155], v[160:163], v[64:79]\n"
      "v_pk_mul_f16 v160, v221, v40 op_sel_hi:[0,1]\n"
      "v_pk_mul_f16 v161, v221, v41 op_sel_hi:[0,1]\n"
      "v_pk_mul_f16 v162, v221, v42 op_sel_hi:[0,1]\n"
      "v_pk_mul_f16 v163, v221, v43 op_sel_hi:[0,1]\n"
      "ds_read_b128 v[204:207], v251 offset:16\n"
      "s_waitcnt lgkmcnt(2)\n"
      "v_mfma_f32_32x32x16_f16 v[48:63], v[144:147], v[156:159], v[48:63]\n"
      "ds_read_b128 v[152:155], v13 offset:43200\n"
      "v_pk_mul_f16 v156, v208, v28 op_sel_hi:[0,1]\n"
      "v_pk_mul_f16 v157, v209, v16 op_sel_hi:[0,1]\n"
      "v_pk_mul_f16 v158, v209, v17 op_sel_hi:[0,1]\n"
      "v_pk_mul_f16 v159, v209, v18 op_sel_hi:[0,1]\n"
      "v_fmac_f32 v112, v80, v30\n"
      "v_mfma_f32_32x32x16_f16 v[64:79], v[144:147], v[160:163], v[64:79]\n"
      "v_pk_mul_f16 v160, v221, v44 op_sel_hi:[0,1]\n"
      "v_pk_mul_f16 v161, v222, v32 op_sel_hi:[0,1]\n"
      "v_pk_mul_f16 v162, v222, v33 op_sel_hi:[0,1]\n"
      "v_pk_mul_f16 v163, v222, v34 op_sel_hi:[0,1]\n"
      "v_fmac_f32 v128, v96, v46\n"
      "s_waitcnt lgkmcnt(2)\n"
      "v_mfma_f32_32x32x16_f16 v[48:63], v[148:151], v[156:159], v[48:63]\n"
      "ds_read_b128 v[144:147], v13 offset:44064\n"
      "v_pk_mul_f16 v156, v209, v19 op_sel_hi:[0,1]\n"
      "v_pk_mul_f16 v157, v209, v20 op_sel_hi:[0,1]\n"
      "v_pk_mul_f16 v158, v209, v21 op_sel_hi:[0,1]\n"
      "v_pk_mul_f16 v159, v209, v22 op_sel_hi:[0,1]\n"
      "v_fmac_f32 v113, v81, v30\n"
      "v_mfma_f32_32x32x16_f16 v[64:79], v[148:151], v[160:163], v[64:79]\n"
      "v_pk_mul_f16 v160, v222, v35 op_sel_hi:[0,1]\n"
      "v_pk_mul_f16 v161, v222, v36 op_sel_hi:[0,1]\n"
      "v_pk_mul_f16 v162, v222, v37 op_sel_hi:[0,1]\n"
      "v_pk_mul_f16 v163, v222, v38 op_sel_hi:[0,1]\n"
      "v_fmac_f32 v129, v97, v46\n"
      "s_waitcnt lgkmcnt(1)\n"
      "v_mfma_f32_32x32x16_f16 v[48:63], v[152:155], v[156:159], v[48:63]\n"
      "ds_read_b128 v[148:151], v13 offset:44928\n"
      "v_pk_mul_f16 v156, v209, v23 op_sel_hi:[0,1]\n"
      "v_pk_mul_f16 v157, v209, v24 op_sel_hi:[0,1]\n"
      "v_pk_mul_f16 v158, v209, v25 op_sel_hi:[0,1]\n"
      "v_pk_mul_f16 v159, v209, v26 op_sel_hi:[0,1]\n"
      "v_fmac_f32 v114, v82, v30\n"
      "v_mfma_f32_32x32x16_f16 v[64:79], v[152:155], v[160:163], v[64:79]\n"
      "v_pk_mul_f16 v160, v222, v39 op_sel_hi:[0,1]\n"
      "v_pk_mul_f16 v161, v222, v40 op_sel_hi:[0,1]\n"
      "v_pk_mul_f16 v162, v222, v41 op_sel_hi:[0,1]\n"
      "v_pk_mul_f16 v163, v222, v42 op_sel_hi:[0,1]\n"
      "v_fmac_f32 v130, v98, v46\n"
      "s_waitcnt lgkmcnt(1)\n"
      "v_mfma_f32_32x32x16_f16 v[48:63], v[144:147], v[156:159], v[48:63]\n"
      "ds_read_b128 v[152:155], v13 offset:45792\n"
      "v_pk_mul_f16 v156, v209, v27 op_sel_hi:[0,1]\n"
      "v_pk_mul_f16 v157, v209, v28 op_sel_hi:[0,1]\n"
      "v_pk_mul_f16 v158, v210, v16 op_sel_hi:[0,1]\n"
      "v_pk_mul_f16 v159, v210, v17 op_sel_hi:[0,1]\n"
      "v_fmac_f32 v115, v83, v30\n"
      "v_mfma_f32_32x32x16_f16 v[64:79], v[144:147], v[160:163], v[64:79]\n"
      "v_pk_mul_f16 v160, v222, v43 op_sel_hi:[0,1]\n"
      "v_pk_mul_f16 v161, v222, v44 op_sel_hi:[0,1]\n"
      "v_pk_mul_f16 v162, v223, v32 op_sel_hi:[0,1]\n"
      "v_pk_mul_f16 v163, v223, v33 op_sel_hi:[0,1]\n"
      "v_fmac_f32 v131, v99, v46\n"
      "s_waitcnt lgkmcnt(1)\n"
      "v_mfma_f32_32x32x16_f16 v[48:63], v[148:151], v[156:159], v[48:63]\n"
      "ds_read_b128 v[144:147], v13 offset:46656\n"
      "v_pk_mul_f16 v156, v210, v18 op_sel_hi:[0,1]\n"
      "v_pk_mul_f16 v157, v210, v19 op_sel_hi:[0,1]\n"
      "v_pk_mul_f16 v158, v210, v20 op_sel_hi:[0,1]\n"
      "v_pk_mul_f16 v159, v210, v21 op_sel_hi:[0,1]\n"
      "v_fmac_f32 v116, v84, v31\n"
      "v_mfma_f32_32x32x16_f16 v[64:79], v[148:151], v[160:163], v[64:79]\n"
      "v_pk_mul_f16 v160, v223, v34 op_sel_hi:[0,1]\n"
      "v_pk_mul_f16 v161, v223, v35 op_sel_hi:[0,1]\n"
      "v_pk_mul_f16 v162, v223, v36 op_sel_hi:[0,1]\n"
      "v_pk_mul_f16 v163, v223, v37 op_sel_hi:[0,1]\n"
      "v_fmac_f32 v132, v100, v47\n"
      "s_waitcnt lgkmcnt(1)\n"
      "v_mfma_f32_32x32x16_f16 v[48:63], v[152:155], v[156:159], v[48:63]\n"
      "ds_read_b128 v[148:151], v13 offset:47520\n"
      "v_pk_mul_f16 v156, v210, v22 op_sel_hi:[0,1]\n"
      "v_pk_mul_f16 v157, v210, v23 op_sel_hi:[0,1]\n"
      "v_pk_mul_f16 v158, v210, v24 op_sel_hi:[0,1]\n"
      "v_pk_mul_f16 v159, v210, v25 op_sel_hi:[0,1]\n"
      "v_fmac_f32 v117, v85, v31\n"
      "v_mfma_f32_32x32x16_f16 v[64:79], v[152:155], v[160:163], v[64:79]\n"
      "v_pk_mul_f16 v160, v223, v38 op_sel_hi:[0,1]\n"
      "v_pk_mul_f16 v161, v223, v39 op_sel_hi:[0,1]\n"
      "v_pk_mul_f16 v162, v223, v40 op_sel_hi:[0,1]\n"
      "v_pk_mul_f16 v163, v223, v41 op_sel_hi:[0,1]\n"
      "v_fmac_f32 v133, v101, v47\n"
      "s_waitcnt lgkmcnt(1)\n"
      "v_mfma_f32_32x32x16_f16 v[48:63], v[144:147], v[156:159], v[48:63]\n"
      "ds_read_b128 v[152:155], v13 offset:48384\n"
      "v_pk_mul_f16 v156, v210, v26 op_sel_hi:[0,1]\n"
      "v_pk_mul_f16 v157, v210, v27 op_sel_hi:[0,1]\n"
      "v_pk_mul_f16 v158, v210, v28 op_sel_hi:[0,1]\n"
      "v_pk_mul_f16 v159, v211, v16 op_sel_hi:[0,1]\n"
      "v_fmac_f32 v118, v86, v31\n"
      "v_mfma_f32_32x32x16_f16 v[64:79], v[144:147], v[160:163], v[64:79]\n"
      "v_pk_mul_f16 v160, v223, v42 op_sel_hi:[0,1]\n"
      "v_pk_mul_f16 v161, v223, v43 op_sel_hi:[0,1]\n"
      "v_pk_mul_f16 v162, v223, v44 op_sel_hi:[0,1]\n"
      "v_pk_mul_f16 v163, v224, v32 op_sel_hi:[0,1]\n"
      "v_fmac_f32 v134, v102, v47\n"
      "s_waitcnt lgkmcnt(1)\n"
      "v_mfma_f32_32x32x16_f16 v[48:63], v[148:151], v[156:159], v[48:63]\n"
      "ds_read_b128 v[144:147], v13 offset:49248\n"
      "v_pk_mul_f16 v156, v211, v17 op_sel_hi:[0,1]\n"
      "v_pk_mul_f16 v157, v211, v18 op_sel_hi:[0,1]\n"
      "v_pk_mul_f16 v158, v211, v19 op_sel_hi:[0,1]\n"
      "v_pk_mul_f16 v159, v211, v20 op_sel_hi:[0,1]\n"
      "v_fmac_f32 v119, v87, v31\n"
      "v_mfma_f32_32x32x16_f16 v[64:79], v[148:151], v[160:163], v[64:79]\n"
      "v_pk_mul_f16 v160, v224, v33 op_sel_hi:[0,1]\n"
      "v_pk_mul_f16 v161, v224, v34 op_sel_hi:[0,1]\n"
      "v_pk_mul_f16 v162, v224, v35 op_sel_hi:[0,1]\n"
      "v_pk_mul_f16 v163, v224, v36 op_sel_hi:[0,1]\n"
      "v_fmac_f32 v135, v103, v47\n"
      "s_waitcnt lgkmcnt(1)\n"
      "v_mfma_f32_32x32x16_f16 v[48:63], v[152:155], v[156:159], v[48:63]\n"
      "ds_read_b128 v[148:151], v13 offset:50112\n"
      "v_pk_mul_f16 v156, v211, v21 op_sel_hi:[0,1]\n"
      "v_pk_mul_f16 v157, v211, v22 op_sel_hi:[0,1]\n"
      "v_pk_mul_f16 v158, v211, v23 op_sel_hi:[0,1]\n"
      "v_pk_mul_f16 v159, v211, v24 op_sel_hi:[0,1]\n"
      "v_fmac_f32 v120, v88, v125\n"
      "v_mfma_f32_32x32x16_f16 v[64:79], v[152:155], v[160:163], v[64:79]\n"
      "v_pk_mul_f16 v160, v224, v37 op_sel_hi:[0,1]\n"
      "v_pk_mul_f16 v161, v224, v38 op_sel_hi:[0,1]\n"
      "v_pk_mul_f16 v162, v224, v39 op_sel_hi:[0,1]\n"
      "v_pk_mul_f16 v163, v224, v40 op_sel_hi:[0,1]\n"
      "v_fmac_f32 v136, v104, v141\n"
      "s_waitcnt lgkmcnt(1)\n"
      "v_mfma_f32_32x32x16_f16 v[48:63], v[144:147], v[156:159], v[48:63]\n"
      "ds_read_b128 v[152:155], v13 offset:50976\n"
      "v_pk_mul_f16 v156, v211, v25 op_sel_hi:[0,1]\n"
      "v_pk_mul_f16 v157, v211, v26 op_sel_hi:[0,1]\n"
      "v_pk_mul_f16 v158, v211, v27 op_sel_hi:[0,1]\n"
      "v_pk_mul_f16 v159, v211, v28 op_sel_hi:[0,1]\n"
      "v_fmac_f32 v121, v89, v125\n"
      "v_mfma_f32_32x32x16_f16 v[64:79], v[144:147], v[160:163], v[64:79]\n"
      "v_pk_mul_f16 v160, v224, v41 op_sel_hi:[0,1]\n"
      "v_pk_mul_f16 v161, v224, v42 op_sel_hi:[0,1]\n"
      "v_pk_mul_f16 v162, v224, v43 op_sel_hi:[0,1]\n"
      "v_pk_mul_f16 v163, v224, v44 op_sel_hi:[0,1]\n"
      "v_readfirstlane_b32 s83, v204\n"
      "s_waitcnt lgkmcnt(1)\n"
      "v_mfma_f32_32x32x16_f16 v[48:63], v[148:151], v[156:159], v[48:63]\n"
      "ds_read_b128 v[144:147], v13 offset:51840\n"
      "v_pk_mul_f16 v156, v212, v16 op_sel_hi:[0,1]\n"
      "v_pk_mul_f16 v157, v212, v17 op_sel_hi:[0,1]\n"
      "v_pk_mul_f16 v158, v212, v18 op_sel_hi:[0,1]\n"
      "v_pk_mul_f16 v159, v212, v19 op_sel_hi:[0,1]\n"
      "v_readfirstlane_b32 s84, v205\n"
      "v_mfma_f32_32x32x16_f16 v[64:79], v[148:151], v[160:163], v[64:79]\n"
      "v_pk_mul_f16 v160, v225, v32 op_sel_hi:[0,1]\n"
      "v_pk_mul_f16 v161, v225, v33 op_sel_hi:[0,1]\n"
      "v_pk_mul_f16 v162, v225, v34 op_sel_hi:[0,1]\n"
      "v_pk_mul_f16 v163, v225, v35 op_sel_hi:[0,1]\n"
      "v_readfirstlane_b32 s85, v206\n"
      "s_waitcnt lgkmcnt(1)\n"
      "v_mfma_f32_32x32x16_f16 v[48:63], v[152:155], v[156:159], v[48:63]\n"
      "ds_read_b128 v[148:151], v13 offset:52704\n"
      "v_pk_mul_f16 v156, v212, v20 op_sel_hi:[0,1]\n"
      "v_pk_mul_f16 v157, v212, v21 op_sel_hi:[0,1]\n"
      "v_pk_mul_f16 v158, v212, v22 op_sel_hi:[0,1]\n"
      "v_pk_mul_f16 v159, v212, v23 op_sel_hi:[0,1]\n"
      "v_fmac_f32 v137, v105, v141\n"
      "v_mfma_f32_32x32x16_f16 v[64:79], v[152:155], v[160:163], v[64:79]\n"
      "v_pk_mul_f16 v160, v225, v36 op_sel_hi:[0,1]\n"
      "v_pk_mul_f16 v161, v225, v37 op_sel_hi:[0,1]\n"
      "v_pk_mul_f16 v162, v225, v38 op_sel_hi:[0,1]\n"
      "v_pk_mul_f16 v163, v225, v39 op_sel_hi:[0,1]\n"
      "v_fmac_f32 v122, v90, v125\n"
      "s_waitcnt lgkmcnt(1)\n"
      "v_mfma_f32_32x32x16_f16 v[48:63], v[144:147], v[156:159], v[48:63]\n"
      "ds_read_b128 v[152:155], v13 offset:53568\n"
      "v_pk_mul_f16 v156, v212, v24 op_sel_hi:[0,1]\n"
      "v_pk_mul_f16 v157, v212, v25 op_sel_hi:[0,1]\n"
      "v_pk_mul_f16 v158, v212, v26 op_sel_hi:[0,1]\n"
      "v_pk_mul_f16 v159, v212, v27 op_sel_hi:[0,1]\n"
      "v_fmac_f32 v138, v106, v141\n"
      "v_mfma_f32_32x32x16_f16 v[64:79], v[144:147], v[160:163], v[64:79]\n"
      "v_pk_mul_f16 v160, v225, v40 op_sel_hi:[0,1]\n"
      "v_pk_mul_f16 v161, v225, v41 op_sel_hi:[0,1]\n"
      "v_pk_mul_f16 v162, v225, v42 op_sel_hi:[0,1]\n"
      "v_pk_mul_f16 v163, v225, v43 op_sel_hi:[0,1]\n"
      "v_fmac_f32 v123, v91, v125\n"
      "s_waitcnt lgkmcnt(1)\n"
      "v_mfma_f32_32x32x16_f16 v[48:63], v[148:151], v[156:159], v[48:63]\n"
      "ds_read_b128 v[144:147], v13 offset:54432\n"
      "v_pk_mul_f16 v156, v212, v28 op_sel_hi:[0,1]\n"
      "v_pk_mul_f16 v157, v213, v16 op_sel_hi:[0,1]\n"
      "v_pk_mul_f16 v158, v213, v17 op_sel_hi:[0,1]\n"
      "v_pk_mul_f16 v159, v213, v18 op_sel_hi:[0,1]\n"
      "v_fmac_f32 v139, v107, v141\n"
      "v_mfma_f32_32x32x16_f16 v[64:79], v[148:151], v[160:163], v[64:79]\n"
      "v_pk_mul_f16 v160, v225, v44 op_sel_hi:[0,1]\n"
      "v_pk_mul_f16 v161, v226, v32 op_sel_hi:[0,1]\n"
      "v_pk_mul_f16 v162, v226, v33 op_sel_hi:[0,1]\n"
      "v_pk_mul_f16 v163, v226, v34 op_sel_hi:[0,1]\n"
      "v_fmac_f32 v124, v92, v126\n"
      "s_waitcnt lgkmcnt(1)\n"
      "v_mfma_f32_32x32x16_f16 v[48:63], v[152:155], v[156:159], v[48:63]\n"
      "ds_read_b128 v[148:151], v13 offset:55296\n"
      "v_pk_mul_f16 v156, v213, v19 op_sel_hi:[0,1]\n"
      "v_pk_mul_f16 v157, v213, v20 op_sel_hi:[0,1]\n"
      "v_pk_mul_f16 v158, v213, v21 op_sel_hi:[0,1]\n"
      "v_pk_mul_f16 v159, v213, v22 op_sel_hi:[0,1]\n"
      "v_fmac_f32 v140, v108, v142\n"
      "v_mfma_f32_32x32x16_f16 v[64:79], v[152:155], v[160:163], v[64:79]\n"
      "v_pk_mul_f16 v160, v226, v35 op_sel_hi:[0,1]\n"
      "v_pk_mul_f16 v161, v226, v36 op_sel_hi:[0,1]\n"
      "v_pk_mul_f16 v162, v226, v37 op_sel_hi:[0,1]\n"
      "v_pk_mul_f16 v163, v226, v38 op_sel_hi:[0,1]\n"
      "v_max3_f32 v242, |v112|, |v113|, |v114|\n"
      "s_waitcnt lgkmcnt(1)\n"
      "v_mfma_f32_32x32x16_f16 v[48:63], v[144:147], v[156:159], v[48:63]\n"
      "ds_read_b128 v[152:155], v13 offset:56160\n"
      "v_pk_mul_f16 v156, v213, v23 op_sel_hi:[0,1]\n"
      "v_pk_mul_f16 v157, v213, v24 op_sel_hi:[0,1]\n"
      "v_pk_mul_f16 v158, v213, v25 op_sel_hi:[0,1]\n"
      "v_pk_mul_f16 v159, v213, v26 op_sel_hi:[0,1]\n"
      "v_max3_f32 v246, |v128|, |v129|, |v130|\n"
      "v_mfma_f32_32x32x16_f16 v[64:79], v[144:147], v[160:163], v[64:79]\n"
      "v_pk_mul_f16 v160, v226, v39 op_sel_hi:[0,1]\n"
      "v_pk_mul_f16 v161, v226, v40 op_sel_hi:[0,1]\n"
      "v_pk_mul_f16 v162, v226, v41 op_sel_hi:[0,1]\n"
      "v_pk_mul_f16 v163, v226, v42 op_sel_hi:[0,1]\n"
      "v_max3_f32 v242, v242, |v115|, |v116|\n"
      "s_waitcnt lgkmcnt(1)\n"
      "v_mfma_f32_32x32x16_f16 v[48:63], v[148:151], v[156:159], v[48:63]\n"
      "ds_read_b128 v[144:147], v13 offset:57024\n"
      "v_pk_mul_f16 v156, v213, v27 op_sel_hi:[0,1]\n"
      "v_pk_mul_f16 v157, v213, v28 op_sel_hi:[0,1]\n"
      "v_pk_mul_f16 v158, v214, v16 op_sel_hi:[0,1]\n"
      "v_pk_mul_f16 v159, v214, v17 op_sel_hi:[0,1]\n"
      "v_max3_f32 v246, v246, |v131|, |v132|\n"
      "v_mfma_f32_32x32x16_f16 v[64:79], v[148:151], v[160:163], v[64:79]\n"
      "v_pk_mul_f16 v160, v226, v43 op_sel_hi:[0,1]\n"
      "v_pk_mul_f16 v161, v226, v44 op_sel_hi:[0,1]\n"
      "v_pk_mul_f16 v162, v227, v32 op_sel_hi:[0,1]\n"
      "v_pk_mul_f16 v163, v227, v33 op_sel_hi:[0,1]\n"
      "v_max3_f32 v242, v242, |v117|, |v118|\n"
      "s_waitcnt lgkmcnt(1)\n"
      "v_mfma_f32_32x32x16_f16 v[48:63], v[152:155], v[156:159], v[48:63]\n"
      "ds_read_b128 v[148:151], v13 offset:57888\n"
      "v_pk_mul_f16 v156, v214, v18 op_sel_hi:[0,1]\n"
      "v_pk_mul_f16 v157, v214, v19 op_sel_hi:[0,1]\n"
      "v_pk_mul_f16 v158, v214, v20 op_sel_hi:[0,1]\n"
      "v_pk_mul_f16 v159, v214, v21 op_sel_hi:[0,1]\n"
      "v_max3_f32 v246, v246, |v133|, |v134|\n"
      "v_mfma_f32_32x32x16_f16 v[64:79], v[152:155], v[160:163], v[64:79]\n"
      "v_pk_mul_f16 v160, v227, v34 op_sel_hi:[0,1]\n"
      "v_pk_mul_f16 v161, v227, v35 op_sel_hi:[0,1]\n"
      "v_pk_mul_f16 v162, v227, v36 op_sel_hi:[0,1]\n"
      "v_pk_mul_f16 v163, v227, v37 op_sel_hi:[0,1]\n"
      "v_max3_f32 v242, v242, |v119|, |v120|\n"
      "s_waitcnt lgkmcnt(1)\n"
      "v_mfma_f32_32x32x16_f16 v[48:63], v[144:147], v[156:159], v[48:63]\n"
      "ds_read_b128 v[152:155], v13 offset:58752\n"
      "v_pk_mul_f16 v156, v214, v22 op_sel_hi:[0,1]\n"
      "v_pk_mul_f16 v157, v214, v23 op_sel_hi:[0,1]\n"
      "v_pk_mul_f16 v158, v214, v24 op_sel_hi:[0,1]\n"
      "v_pk_mul_f16 v159, v214, v25 op_sel_hi:[0,1]\n"
      "v_max3_f32 v246, v246, |v135|, |v136|\n"
      "v_mfma_f32_32x32x16_f16 v[64:79], v[144:147], v[160:163], v[64:79]\n"
      "v_pk_mul_f16 v160, v227, v38 op_sel_hi:[0,1]\n"
      "v_pk_mul_f16 v161, v227, v39 op_sel_hi:[0,1]\n"
      "v_pk_mul_f16 v162, v227, v40 op_sel_hi:[0,1]\n"
      "v_pk_mul_f16 v163, v227, v41 op_sel_hi:[0,1]\n"
      "v_max3_f32 v242, v242, |v121|, |v122|\n"
      "s_waitcnt lgkmcnt(1)\n"
      "v_mfma_f32_32x32x16_f16 v[48:63], v[148:151], v[156:159], v[48:63]\n"
      "ds_read_b128 v[144:147], v13 offset:59616\n"
      "v_pk_mul_f16 v156, v214, v26 op_sel_hi:[0,1]\n"
      "v_pk_mul_f16 v157, v214, v27 op_sel_hi:[0,1]\n"
      "v_pk_mul_f16 v158, v214, v28 op_sel_hi:[0,1]\n"
      "v_pk_mul_f16 v159, v215, v16 op_sel_hi:[0,1]\n"
      "v_max3_f32 v246, v246, |v137|, |v138|\n"
      "v_mfma_f32_32x32x16_f16 v[64:79], v[148:151], v[160:163], v[64:79]\n"
      "v_pk_mul_f16 v160, v227, v42 op_sel_hi:[0,1]\n"
      "v_pk_mul_f16 v161, v227, v43 op_sel_hi:[0,1]\n"
      "v_pk_mul_f16 v162, v227, v44 op_sel_hi:[0,1]\n"
      "v_pk_mul_f16 v163, v228, v32 op_sel_hi:[0,1]\n"
      "v_max3_f32 v242, v242, |v123|, |v124|\n"
      "s_waitcnt lgkmcnt(1)\n"
      "v_mfma_f32_32x32x16_f16 v[48:63], v[152:155], v[156:159], v[48:63]\n"
      "ds_read_b128 v[148:151], v13 offset:60480\n"
      "v_pk_mul_f16 v156, v215, v17 op_sel_hi:[0,1]\n"
      "v_pk_mul_f16 v157, v215, v18 op_sel_hi:[0,1]\n"
      "v_pk_mul_f16 v158, v215, v19 op_sel_hi:[0,1]\n"
      "v_pk_mul_f16 v159, v215, v20 op_sel_hi:[0,1]\n"
      "v_max3_f32 v246, v246, |v139|, |v140|\n"
      "v_mfma_f32_32x32x16_f16 v[64:79], v[152:155], v[160:163], v[64:79]\n"
      "v_pk_mul_f16 v160, v228, v33 op_sel_hi:[0,1]\n"
      "v_pk_mul_f16 v161, v228, v34 op_sel_hi:[0,1]\n"
      "v_pk_mul_f16 v162, v228, v35 op_sel_hi:[0,1]\n"
      "v_pk_mul_f16 v163, v228, v36 op_sel_hi:[0,1]\n"
      "v_mov_b32 v243, v242\n"
      "s_waitcnt lgkmcnt(1)\n"
      "v_mfma_f32_32x32x16_f16 v[48:63], v[144:147], v[156:159], v[48:63]\n"
      "ds_read_b128 v[152:155], v13 offset:61344\n"
      "v_pk_mul_f16 v156, v215, v21 op_sel_hi:[0,1]\n"
      "v_pk_mul_f16 v157, v215, v22 op_sel_hi:[0,1]\n"
      "v_pk_mul_f16 v158, v215, v23 op_sel_hi:[0,1]\n"
      "v_pk_mul_f16 v159, v215, v24 op_sel_hi:[0,1]\n"
      "v_mov_b32 v247, v246\n"
      "v_mfma_f32_32x32x16_f16 v[64:79], v[144:147], v[160:163], v[64:79]\n"
      "v_pk_mul_f16 v160, v228, v37 op_sel_hi:[0,1]\n"
      "v_pk_mul_f16 v161, v228, v38 op_sel_hi:[0,1]\n"
      "v_pk_mul_f16 v162, v228, v39 op_sel_hi:[0,1]\n"
      "v_pk_mul_f16 v163, v228, v40 op_sel_hi:[0,1]\n"
      "v_permlane32_swap_b32 v242, v243\n"
      "s_waitcnt lgkmcnt(1)\n"
      "v_mfma_f32_32x32x16_f16 v[48:63], v[148:151], v[156:159], v[48:63]\n"
      "ds_read_b128 v[144:147], v13 offset:62208\n"
      "v_pk_mul_f16 v156, v215, v25 op_sel_hi:[0,1]\n"
      "v_pk_mul_f16 v157, v215, v26 op_sel_hi:[0,1]\n"
      "v_pk_mul_f16 v158, v215, v27 op_sel_hi:[0,1]\n"
      "v_pk_mul_f16 v159, v215, v28 op_sel_hi:[0,1]\n"
      "v_permlane32_swap_b32 v246, v247\n"
      "v_mfma_f32_32x32x16_f16 v[64:79], v[148:151], v[160:163], v[64:79]\n"
      "v_pk_mul_f16 v160, v228, v41 op_sel_hi:[0,1]\n"
      "v_pk_mul_f16 v161, v228, v42 op_sel_hi:[0,1]\n"
      "v_pk_mul_f16 v162, v228, v43 op_sel_hi:[0,1]\n"
      "v_pk_mul_f16 v163, v228, v44 op_sel_hi:[0,1]\n"
      "v_max3_f32 v242, v242, v243, s81\n"
      "s_waitcnt lgkmcnt(1)\n"
      "v_mfma_f32_32x32x16_f16 v[48:63], v[152:155], v[156:159], v[48:63]\n"
      "ds_read_b128 v[148:151], v13 offset:63072\n"
      "v_pk_mul_f16 v156, v216, v16 op_sel_hi:[0,1]\n"
      "v_pk_mul_f16 v157, v216, v17 op_sel_hi:[0,1]\n"
      "v_pk_mul_f16 v158, v216, v18 op_sel_hi:[0,1]\n"
      "v_pk_mul_f16 v159, v216, v19 op_sel_hi:[0,1]\n"
      "v_max3_f32 v246, v246, v247, s81\n"
      "v_mfma_f32_32x32x16_f16 v[64:79], v[152:155], v[160:163], v[64:79]\n"
      "v_pk_mul_f16 v160, v229, v32 op_sel_hi:[0,1]\n"
      "v_pk_mul_f16 v161, v229, v33 op_sel_hi:[0,1]\n"
      "v_pk_mul_f16 v162, v229, v34 op_sel_hi:[0,1]\n"
      "v_pk_mul_f16 v163, v229, v35 op_sel_hi:[0,1]\n"
      "v_lshrrev_b32 v242, 23, v242\n"
      "s_waitcnt lgkmcnt(1)\n"
      "v_mfma_f32_32x32x16_f16 v[48:63], v[144:147], v[156:159], v[48:63]\n"
      "ds_read_b128 v[152:155], v13 offset:63936\n"
      "v_pk_mul_f16 v156, v216, v20 op_sel_hi:[0,1]\n"
      "v_pk_mul_f16 v157, v216, v21 op_sel_hi:[0,1]\n"
      "v_pk_mul_f16 v158, v216, v22 op_sel_hi:[0,1]\n"
      "v_pk_mul_f16 v159, v216, v23 op_sel_hi:[0,1]\n"
      "v_lshrrev_b32 v246, 23, v246\n"
      "v_mfma_f32_32x32x16_f16 v[64:79], v[144:147], v[160:163], v[64:79]\n"
      "v_pk_mul_f16 v160, v229, v36 op_sel_hi:[0,1]\n"
      "v_pk_mul_f16 v161, v229, v37 op_sel_hi:[0,1]\n"
      "v_pk_mul_f16 v162, v229, v38 op_sel_hi:[0,1]\n"
      "v_pk_mul_f16 v163, v229, v39 op_sel_hi:[0,1]\n"
      "v_mad_i32_i24 v244, v242, v250, s80\n"
      "s_waitcnt lgkmcnt(1)\n"
      "v_mfma_f32_32x32x16_f16 v[48:63], v[148:151], v[156:159], v[48:63]\n"
      "ds_read_b128 v[144:147], v234 offset:0\n"
      "v_pk_mul_f16 v156, v216, v24 op_sel_hi:[0,1]\n"
      "v_pk_mul_f16 v157, v216, v25 op_sel_hi:[0,1]\n"
      "v_pk_mul_f16 v158, v216, v26 op_sel_hi:[0,1]\n"
      "v_pk_mul_f16 v159, v216, v27 op_sel_hi:[0,1]\n"
      "v_mad_i32_i24 v248, v246, v250, s80\n"
      "v_mfma_f32_32x32x16_f16 v[64:79], v[148:151], v[160:163], v[64:79]\n"
      "v_pk_mul_f16 v160, v229, v40 op_sel_hi:[0,1]\n"
      "v_pk_mul_f16 v161, v229, v41 op_sel_hi:[0,1]\n"
      "v_pk_mul_f16 v162, v229, v42 op_sel_hi:[0,1]\n"
      "v_pk_mul_f16 v163, v229, v43 op_sel_hi:[0,1]\n"
      "v_fma_mixlo_f16 v172, v112, v244, 0\n"
      "s_waitcnt lgkmcnt(1)\n"
      "v_mfma_f32_32x32x16_f16 v[48:63], v[152:155], v[156:159], v[48:63]\n"
      "ds_read_b128 v[148:151], v234 offset:864\n"
      "v_pk_mul_f16 v156, v216, v28 op_sel_hi:[0,1]\n"
      "v_pk_mul_f16 v157, v217, v16 op_sel_hi:[0,1]\n"
      "v_pk_mul_f16 v158, v217, v17 op_sel_hi:[0,1]\n"
      "v_pk_mul_f16 v159, v217, v18 op_sel_hi:[0,1]\n"
      "v_fma_mixlo_f16 v185, v128, v248, 0\n"
      "v_mfma_f32_32x32x16_f16 v[64:79], v[152:155], v[160:163], v[64:79]\n"
      "v_pk_mul_f16 v160, v229, v44 op_sel_hi:[0,1]\n"
      "v_pk_mul_f16 v161, v230, v32 op_sel_hi:[0,1]\n"
      "v_pk_mul_f16 v162, v230, v33 op_sel_hi:[0,1]\n"
      "v_pk_mul_f16 v163, v230, v34 op_sel_hi:[0,1]\n"
      "v_fma_mixlo_f16 v173, v113, v244, 0\n"
      "s_waitcnt lgkmcnt(1)\n"
      "v_mfma_f32_32x32x16_f16 v[48:63], v[144:147], v[156:159], v[48:63]\n"
      "ds_read_b128 v[152:155], v234 offset:1728\n"
      "v_pk_mul_f16 v156, v217, v19 op_sel_hi:[0,1]\n"
      "v_pk_mul_f16 v157, v217, v20 op_sel_hi:[0,1]\n"
      "v_pk_mul_f16 v158, v217, v21 op_sel_hi:[0,1]\n"
      "v_pk_mul_f16 v159, v217, v22 op_sel_hi:[0,1]\n"
      "v_fma_mixlo_f16 v186, v129, v248, 0\n"
      "v_mfma_f32_32x32x16_f16 v[64:79], v[144:147], v[160:163], v[64:79]\n"
      "v_pk_mul_f16 v160, v230, v35 op_sel_hi:[0,1]\n"
      "v_pk_mul_f16 v161, v230, v36 op_sel_hi:[0,1]\n"
      "v_pk_mul_f16 v162, v230, v37 op_sel_hi:[0,1]\n"
      "v_pk_mul_f16 v163, v230, v38 op_sel_hi:[0,1]\n"
      "v_fma_mixlo_f16 v174, v114, v244, 0\n"
      "s_waitcnt lgkmcnt(1)\n"
      "v_mfma_f32_32x32x16_f16 v[48:63], v[148:151], v[156:159], v[48:63]\n"
      "ds_read_b128 v[144:147], v234 offset:2592\n"
      "v_pk_mul_f16 v156, v217, v23 op_sel_hi:[0,1]\n"
      "v_pk_mul_f16 v157, v217, v24 op_sel_hi:[0,1]\n"
      "v_pk_mul_f16 v158, v217, v25 op_sel_hi:[0,1]\n"
      "v_pk_mul_f16 v159, v217, v26 op_sel_hi:[0,1]\n"
      "v_fma_mixlo_f16 v187, v130, v248, 0\n"
      "v_mfma_f32_32x32x16_f16 v[64:79], v[148:151], v[160:163], v[64:79]\n"
      "v_pk_mul_f16 v160, v230, v39 op_sel_hi:[0,1]\n"
      "v_pk_mul_f16 v161, v230, v40 op_sel_hi:[0,1]\n"
      "v_pk_mul_f16 v162, v230, v41 op_sel_hi:[0,1]\n"
      "v_pk_mul_f16 v163, v230, v42 op_sel_hi:[0,1]\n"
      "v_fma_mixlo_f16 v175, v115, v244, 0\n"
      "s_waitcnt lgkmcnt(1)\n"
      "v_mfma_f32_32x32x16_f16 v[48:63], v[152:155], v[156:159], v[48:63]\n"
      "ds_read_b128 v[148:151], v234 offset:3456\n"
      "v_pk_mul_f16 v156, v217, v27 op_sel_hi:[0,1]\n"
      "v_pk_mul_f16 v157, v217, v28 op_sel_hi:[0,1]\n"
      "v_pk_mul_f16 v158, v218, v16 op_sel_hi:[0,1]\n"
      "v_pk_mul_f16 v159, v218, v17 op_sel_hi:[0,1]\n"
      "v_fma_mixlo_f16 v188, v131, v248, 0\n"
      "v_mfma_f32_32x32x16_f16 v[64:79], v[152:155], v[160:163], v[64:79]\n"
      "v_pk_mul_f16 v160, v230, v43 op_sel_hi:[0,1]\n"
      "v_pk_mul_f16 v161, v230, v44 op_sel_hi:[0,1]\n"
      "v_pk_mul_f16 v162, v231, v32 op_sel_hi:[0,1]\n"
      "v_pk_mul_f16 v163, v231, v33 op_sel_hi:[0,1]\n"
      "v_fma_mixlo_f16 v176, v116, v244, 0\n"
      "s_waitcnt lgkmcnt(1)\n"
      "v_mfma_f32_32x32x16_f16 v[48:63], v[144:147], v[156:159], v[48:63]\n"
      "ds_read_b128 v[152:155], v234 offset:4320\n"
      "v_pk_mul_f16 v156, v218, v18 op_sel_hi:[0,1]\n"
      "v_pk_mul_f16 v157, v218, v19 op_sel_hi:[0,1]\n"
      "v_pk_mul_f16 v158, v218, v20 op_sel_hi:[0,1]\n"
      "v_pk_mul_f16 v159, v218, v21 op_sel_hi:[0,1]\n"
      "v_fma_mixlo_f16 v189, v132, v248, 0\n"
      "v_mfma_f32_32x32x16_f16 v[64:79], v[144:147], v[160:163], v[64:79]\n"
      "v_pk_mul_f16 v160, v231, v34 op_sel_hi:[0,1]\n"
      "v_pk_mul_f16 v161, v231, v35 op_sel_hi:[0,1]\n"
      "v_pk_mul_f16 v162, v231, v36 op_sel_hi:[0,1]\n"
      "v_pk_mul_f16 v163, v231, v37 op_sel_hi:[0,1]\n"
      "v_fma_mixlo_f16 v177, v117, v244, 0\n"
      "s_waitcnt lgkmcnt(1)\n"
      "v_mfma_f32_32x32x16_f16 v[48:63], v[148:151], v[156:159], v[48:63]\n"
      "ds_read_b128 v[144:147], v234 offset:5184\n"
      "v_pk_mul_f16 v156, v218, v22 op_sel_hi:[0,1]\n"
      "v_pk_mul_f16 v157, v218, v23 op_sel_hi:[0,1]\n"
      "v_pk_mul_f16 v158, v218, v24 op_sel_hi:[0,1]\n"
      "v_pk_mul_f16 v159, v218, v25 op_sel_hi:[0,1]\n"
      "v_fma_mixlo_f16 v190, v133, v248, 0\n"
      "v_mfma_f32_32x32x16_f16 v[64:79], v[148:151], v[160:163], v[64:79]\n"
      "v_pk_mul_f16 v160, v231, v38 op_sel_hi:[0,1]\n"
      "v_pk_mul_f16 v161, v231, v39 op_sel_hi:[0,1]\n"
      "v_pk_mul_f16 v162, v231, v40 op_sel_hi:[0,1]\n"
      "v_pk_mul_f16 v163, v231, v41 op_sel_hi:[0,1]\n"
      "v_fma_mixlo_f16 v178, v118, v244, 0\n"
      "s_waitcnt lgkmcnt(1)\n"
      "v_mfma_f32_32x32x16_f16 v[48:63], v[152:155], v[156:159], v[48:63]\n"
      "ds_read_b128 v[148:151], v234 offset:6048\n"
      "v_pk_mul_f16 v156, v218, v26 op_sel_hi:[0,1]\n"
      "v_pk_mul_f16 v157, v218, v27 op_sel_hi:[0,1]\n"
      "v_pk_mul_f16 v158, v218, v28 op_sel_hi:[0,1]\n"
      "v_pk_mul_f16 v159, v219, v16 op_sel_hi:[0,1]\n"
      "v_fma_mixlo_f16 v191, v134, v248, 0\n"
      "v_mfma_f32_32x32x16_f16 v[64:79], v[152:155], v[160:163], v[64:79]\n"
      "v_pk_mul_f16 v160, v231, v42 op_sel_hi:[0,1]\n"
      "v_pk_mul_f16 v161, v231, v43 op_sel_hi:[0,1]\n"
      "v_pk_mul_f16 v162, v231, v44 op_sel_hi:[0,1]\n"
      "v_pk_mul_f16 v163, v232, v32 op_sel_hi:[0,1]\n"
      "v_fma_mixlo_f16 v179, v119, v244, 0\n"
      "s_waitcnt lgkmcnt(1)\n"
      "v_mfma_f32_32x32x16_f16 v[48:63], v[144:147], v[156:159], v[48:63]\n"
      "ds_read_b128 v[152:155], v234 offset:6912\n"
      "v_pk_mul_f16 v156, v219, v17 op_sel_hi:[0,1]\n"
      "v_pk_mul_f16 v157, v219, v18 op_sel_hi:[0,1]\n"
      "v_pk_mul_f16 v158, v219, v19 op_sel_hi:[0,1]\n"
      "v_pk_mul_f16 v159, v219, v20 op_sel_hi:[0,1]\n"
      "v_fma_mixlo_f16 v192, v135, v248, 0\n"
      "v_mfma_f32_32x32x16_f16 v[64:79], v[144:147], v[160:163], v[64:79]\n"
      "v_pk_mul_f16 v160, v232, v33 op_sel_hi:[0,1]\n"
      "v_pk_mul_f16 v161, v232, v34 op_sel_hi:[0,1]\n"
      "v_pk_mul_f16 v162, v232, v35 op_sel_hi:[0,1]\n"
      "v_pk_mul_f16 v163, v232, v36 op_sel_hi:[0,1]\n"
      "v_fma_mixlo_f16 v180, v120, v244, 0\n"
      "s_waitcnt lgkmcnt(1)\n"
      "v_mfma_f32_32x32x16_f16 v[48:63], v[148:151], v[156:159], v[48:63]\n"
      "ds_read_b128 v[144:147], v234 offset:7776\n"
      "v_pk_mul_f16 v156, v219, v21 op_sel_hi:[0,1]\n"
      "v_pk_mul_f16 v157, v219, v22 op_sel_hi:[0,1]\n"
      "v_pk_mul_f16 v158, v219, v23 op_sel_hi:[0,1]\n"
      "v_pk_mul_f16 v159, v219, v24 op_sel_hi:[0,1]\n"
      "v_fma_mixlo_f16 v193, v136, v248, 0\n"
      "v_mfma_f32_32x32x16_f16 v[64:79], v[148:151], v[160:163], v[64:79]\n"
      "v_pk_mul_f16 v160, v232, v37 op_sel_hi:[0,1]\n"
      "v_pk_mul_f16 v161, v232, v38 op_sel_hi:[0,1]\n"
      "v_pk_mul_f16 v162, v232, v39 op_sel_hi:[0,1]\n"
      "v_pk_mul_f16 v163, v232, v40 op_sel_hi:[0,1]\n"
      "v_fma_mixlo_f16 v181, v121, v244, 0\n"
      "s_waitcnt lgkmcnt(1)\n"
      "v_mfma_f32_32x32x16_f16 v[48:63], v[152:155], v[156:159], v[48:63]\n"
      "ds_read_b128 v[148:151], v234 offset:8640\n"
      "v_pk_mul_f16 v156, v219, v25 op_sel_hi:[0,1]\n"
      "v_pk_mul_f16 v157, v219, v26 op_sel_hi:[0,1]\n"
      "v_pk_mul_f16 v158, v219, v27 op_sel_hi:[0,1]\n"
      "v_pk_mul_f16 v159, v219, v28 op_sel_hi:[0,1]\n"
      "v_fma_mixlo_f16 v194, v137, v248, 0\n"
      "v_mfma_f32_32x32x16_f16 v[64:79], v[152:155], v[160:163], v[64:79]\n"
      "v_pk_mul_f16 v160, v232, v41 op_sel_hi:[0,1]\n"
      "v_pk_mul_f16 v161, v232, v42 op_sel_hi:[0,1]\n"
      "v_pk_mul_f16 v162, v232, v43 op_sel_hi:[0,1]\n"
      "v_pk_mul_f16 v163, v232, v44 op_sel_hi:[0,1]\n"
      "v_fma_mixlo_f16 v182, v122, v244, 0\n"
      "s_waitcnt lgkmcnt(1)\n"
      "v_mfma_f32_32x32x16_f16 v[48:63], v[144:147], v[156:159], v[48:63]\n"
      "ds_read_b128 v[152:155], v234 offset:9504\n"
      "v_pk_mul_f16 v156, v220, v16 op_sel_hi:[0,1]\n"
      "v_pk_mul_f16 v157, v220, v17 op_sel_hi:[0,1]\n"
      "v_pk_mul_f16 v158, v220, v18 op_sel_hi:[0,1]\n"
      "v_pk_mul_f16 v159, v220, v19 op_sel_hi:[0,1]\n"
      "v_fma_mixlo_f16 v195, v138, v248, 0\n"
      "v_mfma_f32_32x32x16_f16 v[64:79], v[144:147], v[160:163], v[64:79]\n"
      "v_pk_mul_f16 v160, v233, v32 op_sel_hi:[0,1]\n"
      "v_pk_mul_f16 v161, v233, v33 op_sel_hi:[0,1]\n"
      "v_pk_mul_f16 v162, v233, v34 op_sel_hi:[0,1]\n"
      "v_pk_mul_f16 v163, v233, v35 op_sel_hi:[0,1]\n"
      "v_fma_mixlo_f16 v183, v123, v244, 0\n"
      "s_waitcnt lgkmcnt(1)\n"
      "v_mfma_f32_32x32x16_f16 v[48:63], v[148:151], v[156:159], v[48:63]\n"
      "ds_read_b128 v[144:147], v234 offset:10368\n"
      "v_pk_mul_f16 v156, v220, v20 op_sel_hi:[0,1]\n"
      "v_pk_mul_f16 v157, v220, v21 op_sel_hi:[0,1]\n"
      "v_pk_mul_f16 v158, v220, v22 op_sel_hi:[0,1]\n"
      "v_pk_mul_f16 v159, v220, v23 op_sel_hi:[0,1]\n"
      "v_fma_mixlo_f16 v196, v139, v248, 0\n"
      "v_mfma_f32_32x32x16_f16 v[64:79], v[148:151], v[160:163], v[64:79]\n"
      "v_pk_mul_f16 v160, v233, v36 op_sel_hi:[0,1]\n"
      "v_pk_mul_f16 v161, v233, v37 op_sel_hi:[0,1]\n"
      "v_pk_mul_f16 v162, v233, v38 op_sel_hi:[0,1]\n"
      "v_pk_mul_f16 v163, v233, v39 op_sel_hi:[0,1]\n"
      "v_fma_mixlo_f16 v184, v124, v244, 0\n"
      "s_waitcnt lgkmcnt(1)\n"
      "v_mfma_f32_32x32x16_f16 v[48:63], v[152:155], v[156:159], v[48:63]\n"
      "ds_read_b128 v[148:151], v234 offset:11232\n"
      "v_pk_mul_f16 v156, v220, v24 op_sel_hi:[0,1]\n"
      "v_pk_mul_f16 v157, v220, v25 op_sel_hi:[0,1]\n"
      "v_pk_mul_f16 v158, v220, v26 op_sel_hi:[0,1]\n"
      "v_pk_mul_f16 v159, v220, v27 op_sel_hi:[0,1]\n"
      "v_fma_mixlo_f16 v197, v140, v248, 0\n"
      "v_mfma_f32_32x32x16_f16 v[64:79], v[152:155], v[160:163], v[64:79]\n"
      "v_pk_mul_f16 v160, v233, v40 op_sel_hi:[0,1]\n"
      "v_pk_mul_f16 v161, v233, v41 op_sel_hi:[0,1]\n"
      "v_pk_mul_f16 v162, v233, v42 op_sel_hi:[0,1]\n"
      "v_pk_mul_f16 v163, v233, v43 op_sel_hi:[0,1]\n"
      "v_add_u32 v238, v242, v29\n"
      "s_waitcnt lgkmcnt(1)\n"
      "v_mfma_f32_32x32x16_f16 v[48:63], v[144:147], v[156:159], v[48:63]\n"
      "s_barrier\n"
      "ds_read_b128 v[152:155], v234 offset:12096\n"
      "v_pk_mul_f16 v156, v220, v28 op_sel_hi:[0,1]\n"
      "v_add_u32 v239, v246, v45\n"
      "v_pk_mul_f16 v164, v233, v44 op_sel_hi:[0,1]\n"
      "v_pk_mul_f16 v168, v172, v16 op_sel_hi:[0,1]\n"
      "v_mfma_f32_32x32x16_f16 v[64:79], v[144:147], v[160:163], v[64:79]\n"
      "v_pk_mul_f16 v169, v172, v17 op_sel_hi:[0,1]\n"
      "v_pk_mul_f16 v170, v172, v18 op_sel_hi:[0,1]\n"
      "v_pk_mul_f16 v171, v172, v19 op_sel_hi:[0,1]\n"
      "v_pk_mul_f16 v160, v185, v32 op_sel_hi:[0,1]\n"
      "s_waitcnt lgkmcnt(1)\n"
      "v_mfma_f32_32x32x16_f16 v[48:63], v[148:151], v[156:159], v[48:63]\n"
      "ds_read_b128 v[144:147], v234 offset:12960\n"
      "v_pk_mul_f16 v161, v185, v33 op_sel_hi:[0,1]\n"
      "v_pk_mul_f16 v162, v185, v34 op_sel_hi:[0,1]\n"
      "v_pk_mul_f16 v163, v185, v35 op_sel_hi:[0,1]\n"
      "v_pk_mul_f16 v156, v172, v20 op_sel_hi:[0,1]\n"
      "v_mfma_f32_32x32x16_f16 v[64:79], v[148:151], v[164:167], v[64:79]\n"
      "v_pk_mul_f16 v157, v172, v21 op_sel_hi:[0,1]\n"
      "v_pk_mul_f16 v158, v172, v22 op_sel_hi:[0,1]\n"
      "v_pk_mul_f16 v159, v172, v23 op_sel_hi:[0,1]\n"
      "v_pk_mul_f16 v164, v185, v36 op_sel_hi:[0,1]\n"
      "s_waitcnt lgkmcnt(1)\n"
      "v_mfma_f32_32x32x16_f16 v[80:95], v[152:155], v[168:171], 0\n"
      "ds_read_b128 v[148:151], v234 offset:13824\n"
      "v_pk_mul_f16 v165, v185, v37 op_sel_hi:[0,1]\n"
      "v_pk_mul_f16 v166, v185, v38 op_sel_hi:[0,1]\n"
      "v_pk_mul_f16 v167, v185, v39 op_sel_hi:[0,1]\n"
      "v_pk_mul_f16 v168, v172, v24 op_sel_hi:[0,1]\n"
      "v_mfma_f32_32x32x16_f16 v[96:111], v[152:155], v[160:163], 0\n"
      "v_pk_mul_f16 v169, v172, v25 op_sel_hi:[0,1]\n"
      "v_pk_mul_f16 v170, v172, v26 op_sel_hi:[0,1]\n"
      "v_pk_mul_f16 v171, v172, v27 op_sel_hi:[0,1]\n"
      "v_max3_f32 v242, |v48|, |v49|, |v50|\n"
      "v_max3_f32 v246, |v64|, |v65|, |v66|\n"
      "s_waitcnt lgkmcnt(1)\n"
      "v_mfma_f32_32x32x16_f16 v[80:95], v[144:147], v[156:159], v[80:95]\n"
      "ds_read_b128 v[152:155], v234 offset:14688\n"
      "v_max3_f32 v242, v242, |v51|, |v52|\n"
      "v_max3_f32 v246, v246, |v67|, |v68|\n"
      "v_max3_f32 v242, v242, |v53|, |v54|\n"
      "v_max3_f32 v246, v246, |v69|, |v70|\n"
      "v_max3_f32 v242, v242, |v55|, |v56|\n"
      "v_mfma_f32_32x32x16_f16 v[96:111], v[144:147], v[164:167], v[96:111]\n"
      "v_pk_mul_f16 v156, v185, v40 op_sel_hi:[0,1]\n"
      "v_pk_mul_f16 v157, v185, v41 op_sel_hi:[0,1]\n"
      "v_pk_mul_f16 v158, v185, v42 op_sel_hi:[0,1]\n"
      "v_pk_mul_f16 v159, v185, v43 op_sel_hi:[0,1]\n"
      "v_max3_f32 v246, v246, |v71|, |v72|\n"
      "s_waitcnt lgkmcnt(1)\n"
      "v_mfma_f32_32x32x16_f16 v[80:95], v[148:151], v[168:171], v[80:95]\n"
      "ds_read_b128 v[144:147], v234 offset:15552\n"
      "v_pk_mul_f16 v160, v172, v28 op_sel_hi:[0,1]\n"
      "v_pk_mul_f16 v161, v173, v16 op_sel_hi:[0,1]\n"
      "v_pk_mul_f16 v162, v173, v17 op_sel_hi:[0,1]\n"
      "v_pk_mul_f16 v163, v173, v18 op_sel_hi:[0,1]\n"
      "v_max3_f32 v242, v242, |v57|, |v58|\n"
      "v_mfma_f32_32x32x16_f16 v[96:111], v[148:151], v[156:159], v[96:111]\n"
      "v_pk_mul_f16 v156, v185, v44 op_sel_hi:[0,1]\n"
      "v_pk_mul_f16 v157, v186, v32 op_sel_hi:[0,1]\n"
      "v_pk_mul_f16 v158, v186, v33 op_sel_hi:[0,1]\n"
      "v_pk_mul_f16 v159, v186, v34 op_sel_hi:[0,1]\n"
      "v_max3_f32 v246, v246, |v73|, |v74|\n"
      "s_waitcnt lgkmcnt(1)\n"
      "v_mfma_f32_32x32x16_f16 v[80:95], v[152:155], v[160:163], v[80:95]\n"
      "ds_read_b128 v[148:151], v234 offset:16416\n"
      "v_pk_mul_f16 v160, v173, v19 op_sel_hi:[0,1]\n"
      "v_pk_mul_f16 v161, v173, v20 op_sel_hi:[0,1]\n"
      "v_pk_mul_f16 v162, v173, v21 op_sel_hi:[0,1]\n"
      "v_pk_mul_f16 v163, v173, v22 op_sel_hi:[0,1]\n"
      "v_max3_f32 v242, v242, |v59|, |v60|\n"
      "v_mfma_f32_32x32x16_f16 v[96:111], v[152:155], v[156:159], v[96:111]\n"
      "v_pk_mul_f16 v156, v186, v35 op_sel_hi:[0,1]\n"
      "v_pk_mul_f16 v157, v186, v36 op_sel_hi:[0,1]\n"
      "v_pk_mul_f16 v158, v186, v37 op_sel_hi:[0,1]\n"
      "v_pk_mul_f16 v159, v186, v38 op_sel_hi:[0,1]\n"
      "v_max3_f32 v246, v246, |v75|, |v76|\n"
      "s_waitcnt lgkmcnt(1)\n"
      "v_mfma_f32_32x32x16_f16 v[80:95], v[144:147], v[160:163], v[80:95]\n"
      "ds_read_b128 v[152:155], v234 offset:17280\n"
      "v_pk_mul_f16 v160, v173, v23 op_sel_hi:[0,1]\n"
      "v_pk_mul_f16 v161, v173, v24 op_sel_hi:[0,1]\n"
      "v_pk_mul_f16 v162, v173, v25 op_sel_hi:[0,1]\n"
      "v_pk_mul_f16 v163, v173, v26 op_sel_hi:[0,1]\n"
      "v_mov_b32 v243, v242\n"
      "v_mfma_f32_32x32x16_f16 v[96:111], v[144:147], v[156:159], v[96:111]\n"
      "v_pk_mul_f16 v156, v186, v39 op_sel_hi:[0,1]\n"
      "v_pk_mul_f16 v157, v186, v40 op_sel_hi:[0,1]\n"
      "v_pk_mul_f16 v158, v186, v41 op_sel_hi:[0,1]\n"
      "v_pk_mul_f16 v159, v186, v42 op_sel_hi:[0,1]\n"
      "v_mov_b32 v247, v246\n"
      "s_waitcnt lgkmcnt(1)\n"
      "v_mfma_f32_32x32x16_f16 v[80:95], v[148:151], v[160:163], v[80:95]\n"
      "ds_read_b128 v[144:147], v234 offset:18144\n"
      "v_pk_mul_f16 v160, v173, v27 op_sel_hi:[0,1]\n"
      "v_pk_mul_f16 v161, v173, v28 op_sel_hi:[0,1]\n"
      "v_pk_mul_f16 v162, v174, v16 op_sel_hi:[0,1]\n"
      "v_pk_mul_f16 v163, v174, v17 op_sel_hi:[0,1]\n"
      "v_add_u32 v245, s83, v236\n"
      "v_mfma_f32_32x32x16_f16 v[96:111], v[148:151], v[156:159], v[96:111]\n"
      "v_pk_mul_f16 v156, v186, v43 op_sel_hi:[0,1]\n"
      "v_pk_mul_f16 v157, v186, v44 op_sel_hi:[0,1]\n"
      "v_pk_mul_f16 v158, v187, v32 op_sel_hi:[0,1]\n"
      "v_pk_mul_f16 v159, v187, v33 op_sel_hi:[0,1]\n"
      "v_add_u32 v249, s83, v237\n"
      "s_waitcnt lgkmcnt(1)\n"
      "v_mfma_f32_32x32x16_f16 v[80:95], v[152:155], v[160:163], v[80:95]\n"
      "ds_read_b128 v[148:151], v234 offset:19008\n"
      "v_pk_mul_f16 v160, v174, v18 op_sel_hi:[0,1]\n"
      "v_pk_mul_f16 v161, v174, v19 op_sel_hi:[0,1]\n"
      "v_pk_mul_f16 v162, v174, v20 op_sel_hi:[0,1]\n"
      "v_pk_mul_f16 v163, v174, v21 op_sel_hi:[0,1]\n"
      "v_ldexp_f32 v245, v61, v245\n"
      "v_mfma_f32_32x32x16_f16 v[96:111], v[152:155], v[156:159], v[96:111]\n"
      "v_pk_mul_f16 v156, v187, v34 op_sel_hi:[0,1]\n"
      "v_pk_mul_f16 v157, v187, v35 op_sel_hi:[0,1]\n"
      "v_pk_mul_f16 v158, v187, v36 op_sel_hi:[0,1]\n"
      "v_pk_mul_f16 v159, v187, v37 op_sel_hi:[0,1]\n"
      "v_ldexp_f32 v249, v77, v249\n"
      "s_waitcnt lgkmcnt(1)\n"
      "v_mfma_f32_32x32x16_f16 v[80:95], v[144:147], v[160:163], v[80:95]\n"
      "ds_read_b128 v[152:155], v234 offset:19872\n"
      "v_pk_mul_f16 v160, v174, v22 op_sel_hi:[0,1]\n"
      "v_pk_mul_f16 v161, v174, v23 op_sel_hi:[0,1]\n"
      "v_pk_mul_f16 v162, v174, v24 op_sel_hi:[0,1]\n"
      "v_pk_mul_f16 v163, v174, v25 op_sel_hi:[0,1]\n"
      "v_add_f32 v240, v240, v245\n"
      "v_mfma_f32_32x32x16_f16 v[96:111], v[144:147], v[156:159], v[96:111]\n"
      "v_pk_mul_f16 v156, v187, v38 op_sel_hi:[0,1]\n"
      "v_pk_mul_f16 v157, v187, v39 op_sel_hi:[0,1]\n"
      "v_pk_mul_f16 v158, v187, v40 op_sel_hi:[0,1]\n"
      "v_pk_mul_f16 v159, v187, v41 op_sel_hi:[0,1]\n"
      "v_add_f32 v241, v241, v249\n"
      "s_waitcnt lgkmcnt(1)\n"
      "v_mfma_f32_32x32x16_f16 v[80:95], v[148:151], v[160:163], v[80:95]\n"
      "ds_read_b128 v[144:147], v234 offset:20736\n"
      "v_pk_mul_f16 v160, v174, v26 op_sel_hi:[0,1]\n"
      "v_pk_mul_f16 v161, v174, v27 op_sel_hi:[0,1]\n"
      "v_pk_mul_f16 v162, v174, v28 op_sel_hi:[0,1]\n"
      "v_pk_mul_f16 v163, v175, v16 op_sel_hi:[0,1]\n"
      "v_permlane32_swap_b32 v242, v243\n"
      "v_mfma_f32_32x32x16_f16 v[96:111], v[148:151], v[156:159], v[96:111]\n"
      "v_pk_mul_f16 v156, v187, v42 op_sel_hi:[0,1]\n"
      "v_pk_mul_f16 v157, v187, v43 op_sel_hi:[0,1]\n"
      "v_pk_mul_f16 v158, v187, v44 op_sel_hi:[0,1]\n"
      "v_pk_mul_f16 v159, v188, v32 op_sel_hi:[0,1]\n"
      "v_permlane32_swap_b32 v246, v247\n"
      "s_waitcnt lgkmcnt(1)\n"
      "v_mfma_f32_32x32x16_f16 v[80:95], v[152:155], v[160:163], v[80:95]\n"
      "ds_read_b128 v[148:151], v234 offset:21600\n"
      "v_pk_mul_f16 v160, v175, v17 op_sel_hi:[0,1]\n"
      "v_pk_mul_f16 v161, v175, v18 op_sel_hi:[0,1]\n"
      "v_pk_mul_f16 v162, v175, v19 op_sel_hi:[0,1]\n"
      "v_pk_mul_f16 v163, v175, v20 op_sel_hi:[0,1]\n"
      "v_max3_f32 v242, v242, v243, s81\n"
      "v_mfma_f32_32x32x16_f16 v[96:111], v[152:155], v[156:159], v[96:111]\n"
      "v_pk_mul_f16 v156, v188, v33 op_sel_hi:[0,1]\n"
      "v_pk_mul_f16 v157, v188, v34 op_sel_hi:[0,1]\n"
      "v_pk_mul_f16 v158, v188, v35 op_sel_hi:[0,1]\n"
      "v_pk_mul_f16 v159, v188, v36 op_sel_hi:[0,1]\n"
      "v_max3_f32 v246, v246, v247, s81\n"
      "s_waitcnt lgkmcnt(1)\n"
      "v_mfma_f32_32x32x16_f16 v[80:95], v[144:147], v[160:163], v[80:95]\n"
      "ds_read_b128 v[152:155], v234 offset:22464\n"
      "v_pk_mul_f16 v160, v175, v21 op_sel_hi:[0,1]\n"
      "v_pk_mul_f16 v161, v175, v22 op_sel_hi:[0,1]\n"
      "v_pk_mul_f16 v162, v175, v23 op_sel_hi:[0,1]\n"
      "v_pk_mul_f16 v163, v175, v24 op_sel_hi:[0,1]\n"
      "v_lshrrev_b32 v242, 23, v242\n"
      "v_mfma_f32_32x32x16_f16 v[96:111], v[144:147], v[156:159], v[96:111]\n"
      "v_pk_mul_f16 v156, v188, v37 op_sel_hi:[0,1]\n"
      "v_pk_mul_f16 v157, v188, v38 op_sel_hi:[0,1]\n"
      "v_pk_mul_f16 v158, v188, v39 op_sel_hi:[0,1]\n"
      "v_pk_mul_f16 v159, v188, v40 op_sel_hi:[0,1]\n"
      "v_lshrrev_b32 v246, 23, v246\n"
      "s_waitcnt lgkmcnt(1)\n"
      "v_mfma_f32_32x32x16_f16 v[80:95], v[148:151], v[160:163], v[80:95]\n"
      "ds_read_b128 v[144:147], v234 offset:23328\n"
      "v_pk_mul_f16 v160, v175, v25 op_sel_hi:[0,1]\n"
      "v_pk_mul_f16 v161, v175, v26 op_sel_hi:[0,1]\n"
      "v_pk_mul_f16 v162, v175, v27 op_sel_hi:[0,1]\n"
      "v_pk_mul_f16 v163, v175, v28 op_sel_hi:[0,1]\n"
      "v_mad_i32_i24 v244, v242, v250, s80\n"
      "v_mfma_f32_32x32x16_f16 v[96:111], v[148:151], v[156:159], v[96:111]\n"
      "v_pk_mul_f16 v156, v188, v41 op_sel_hi:[0,1]\n"
      "v_pk_mul_f16 v157, v188, v42 op_sel_hi:[0,1]\n"
      "v_pk_mul_f16 v158, v188, v43 op_sel_hi:[0,1]\n"
      "v_pk_mul_f16 v159, v188, v44 op_sel_hi:[0,1]\n"
      "v_mad_i32_i24 v248, v246, v250, s80\n"
      "s_waitcnt lgkmcnt(1)\n"
      "v_mfma_f32_32x32x16_f16 v[80:95], v[152:155], v[160:163], v[80:95]\n"
      "ds_read_b128 v[148:151], v234 offset:24192\n"
      "v_pk_mul_f16 v160, v176, v16 op_sel_hi:[0,1]\n"
      "v_pk_mul_f16 v161, v176, v17 op_sel_hi:[0,1]\n"
      "v_pk_mul_f16 v162, v176, v18 op_sel_hi:[0,1]\n"
      "v_pk_mul_f16 v163, v176, v19 op_sel_hi:[0,1]\n"
      "v_fma_mixlo_f16 v208, v48, v244, 0\n"
      "v_mfma_f32_32x32x16_f16 v[96:111], v[152:155], v[156:159], v[96:111]\n"
      "v_pk_mul_f16 v156, v189, v32 op_sel_hi:[0,1]\n"
      "v_pk_mul_f16 v157, v189, v33 op_sel_hi:[0,1]\n"
      "v_pk_mul_f16 v158, v189, v34 op_sel_hi:[0,1]\n"
      "v_pk_mul_f16 v159, v189, v35 op_sel_hi:[0,1]\n"
      "v_fma_mixlo_f16 v221, v64, v248, 0\n"
      "s_waitcnt lgkmcnt(1)\n"
      "v_mfma_f32_32x32x16_f16 v[80:95], v[144:147], v[160:163], v[80:95]\n"
      "ds_read_b128 v[152:155], v234 offset:25056\n"
      "v_pk_mul_f16 v160, v176, v20 op_sel_hi:[0,1]\n"
      "v_pk_mul_f16 v161, v176, v21 op_sel_hi:[0,1]\n"
      "v_pk_mul_f16 v162, v176, v22 op_sel_hi:[0,1]\n"
      "v_pk_mul_f16 v163, v176, v23 op_sel_hi:[0,1]\n"
      "v_fma_mixlo_f16 v209, v49, v244, 0\n"
      "v_mfma_f32_32x32x16_f16 v[96:111], v[144:147], v[156:159], v[96:111]\n"
      "v_pk_mul_f16 v156, v189, v36 op_sel_hi:[0,1]\n"
      "v_pk_mul_f16 v157, v189, v37 op_sel_hi:[0,1]\n"
      "v_pk_mul_f16 v158, v189, v38 op_sel_hi:[0,1]\n"
      "v_pk_mul_f16 v159, v189, v39 op_sel_hi:[0,1]\n"
      "v_fma_mixlo_f16 v222, v65, v248, 0\n"
      "s_waitcnt lgkmcnt(1)\n"
      "v_mfma_f32_32x32x16_f16 v[80:95], v[148:151], v[160:163], v[80:95]\n"
      "ds_read_b128 v[144:147], v234 offset:25920\n"
      "v_pk_mul_f16 v160, v176, v24 op_sel_hi:[0,1]\n"
      "v_pk_mul_f16 v161, v176, v25 op_sel_hi:[0,1]\n"
      "v_pk_mul_f16 v162, v176, v26 op_sel_hi:[0,1]\n"
      "v_pk_mul_f16 v163, v176, v27 op_sel_hi:[0,1]\n"
      "v_fma_mixlo_f16 v210, v50, v244, 0\n"
      "v_mfma_f32_32x32x16_f16 v[96:111], v[148:151], v[156:159], v[96:111]\n"
      "v_pk_mul_f16 v156, v189, v40 op_sel_hi:[0,1]\n"
      "v_pk_mul_f16 v157, v189, v41 op_sel_hi:[0,1]\n"
      "v_pk_mul_f16 v158, v189, v42 op_sel_hi:[0,1]\n"
      "v_pk_mul_f16 v159, v189, v43 op_sel_hi:[0,1]\n"
      "v_fma_mixlo_f16 v223, v66, v248, 0\n"
      "s_waitcnt lgkmcnt(1)\n"
      "v_mfma_f32_32x32x16_f16 v[80:95], v[152:155], v[160:163], v[80:95]\n"
      "ds_read_b128 v[148:151], v234 offset:26784\n"
      "v_pk_mul_f16 v160, v176, v28 op_sel_hi:[0,1]\n"
      "v_pk_mul_f16 v161, v177, v16 op_sel_hi:[0,1]\n"
      "v_pk_mul_f16 v162, v177, v17 op_sel_hi:[0,1]\n"
      "v_pk_mul_f16 v163, v177, v18 op_sel_hi:[0,1]\n"
      "v_fma_mixlo_f16 v211, v51, v244, 0\n"
      "v_mfma_f32_32x32x16_f16 v[96:111], v[152:155], v[156:159], v[96:111]\n"
      "v_pk_mul_f16 v156, v189, v44 op_sel_hi:[0,1]\n"
      "v_pk_mul_f16 v157, v190, v32 op_sel_hi:[0,1]\n"
      "v_pk_mul_f16 v158, v190, v33 op_sel_hi:[0,1]\n"
      "v_pk_mul_f16 v159, v190, v34 op_sel_hi:[0,1]\n"
      "v_fma_mixlo_f16 v224, v67, v248, 0\n"
      "s_waitcnt lgkmcnt(1)\n"
      "v_mfma_f32_32x32x16_f16 v[80:95], v[144:147], v[160:163], v[80:95]\n"
      "ds_read_b128 v[152:155], v234 offset:27648\n"
      "v_pk_mul_f16 v160, v177, v19 op_sel_hi:[0,1]\n"
      "v_pk_mul_f16 v161, v177, v20 op_sel_hi:[0,1]\n"
      "v_pk_mul_f16 v162, v177, v21 op_sel_hi:[0,1]\n"
      "v_pk_mul_f16 v163, v177, v22 op_sel_hi:[0,1]\n"
      "v_fma_mixlo_f16 v212, v52, v244, 0\n"
      "v_mfma_f32_32x32x16_f16 v[96:111], v[144:147], v[156:159], v[96:111]\n"
      "v_pk_mul_f16 v156, v190, v35 op_sel_hi:[0,1]\n"
      "v_pk_mul_f16 v157, v190, v36 op_sel_hi:[0,1]\n"
      "v_pk_mul_f16 v158, v190, v37 op_sel_hi:[0,1]\n"
      "v_pk_mul_f16 v159, v190, v38 op_sel_hi:[0,1]\n"
      "v_fma_mixlo_f16 v225, v68, v248, 0\n"
      "s_waitcnt lgkmcnt(1)\n"
      "v_mfma_f32_32x32x16_f16 v[80:95], v[148:151], v[160:163], v[80:95]\n"
      "ds_read_b128 v[144:147], v234 offset:28512\n"
      "v_pk_mul_f16 v160, v177, v23 op_sel_hi:[0,1]\n"
      "v_pk_mul_f16 v161, v177, v24 op_sel_hi:[0,1]\n"
      "v_pk_mul_f16 v162, v177, v25 op_sel_hi:[0,1]\n"
      "v_pk_mul_f16 v163, v177, v26 op_sel_hi:[0,1]\n"
      "v_fma_mixlo_f16 v213, v53, v244, 0\n"
      "v_mfma_f32_32x32x16_f16 v[96:111], v[148:151], v[156:159], v[96:111]\n"
      "v_pk_mul_f16 v156, v190, v39 op_sel_hi:[0,1]\n"
      "v_pk_mul_f16 v157, v190, v40 op_sel_hi:[0,1]\n"
      "v_pk_mul_f16 v158, v190, v41 op_sel_hi:[0,1]\n"
      "v_pk_mul_f16 v159, v190, v42 op_sel_hi:[0,1]\n"
      "v_fma_mixlo_f16 v226, v69, v248, 0\n"
      "s_waitcnt lgkmcnt(1)\n"
      "v_mfma_f32_32x32x16_f16 v[80:95], v[152:155], v[160:163], v[80:95]\n"
      "ds_read_b128 v[148:151], v234 offset:29376\n"
      "v_pk_mul_f16 v160, v177, v27 op_sel_hi:[0,1]\n"
      "v_pk_mul_f16 v161, v177, v28 op_sel_hi:[0,1]\n"
      "v_pk_mul_f16 v162, v178, v16 op_sel_hi:[0,1]\n"
      "v_pk_mul_f16 v163, v178, v17 op_sel_hi:[0,1]\n"
      "v_fma_mixlo_f16 v214, v54, v244, 0\n"
      "v_mfma_f32_32x32x16_f16 v[96:111], v[152:155], v[156:159], v[96:111]\n"
      "v_pk_mul_f16 v156, v190, v43 op_sel_hi:[0,1]\n"
      "v_pk_mul_f16 v157, v190, v44 op_sel_hi:[0,1]\n"
      "v_pk_mul_f16 v158, v191, v32 op_sel_hi:[0,1]\n"
      "v_pk_mul_f16 v159, v191, v33 op_sel_hi:[0,1]\n"
      "v_fma_mixlo_f16 v227, v70, v248, 0\n"
      "s_waitcnt lgkmcnt(1)\n"
      "v_mfma_f32_32x32x16_f16 v[80:95], v[144:147], v[160:163], v[80:95]\n"
      "ds_read_b128 v[152:155], v234 offset:30240\n"
      "v_pk_mul_f16 v160, v178, v18 op_sel_hi:[0,1]\n"
      "v_pk_mul_f16 v161, v178, v19 op_sel_hi:[0,1]\n"
      "v_pk_mul_f16 v162, v178, v20 op_sel_hi:[0,1]\n"
      "v_pk_mul_f16 v163, v178, v21 op_sel_hi:[0,1]\n"
      "v_fma_mixlo_f16 v215, v55, v244, 0\n"
      "v_mfma_f32_32x32x16_f16 v[96:111], v[144:147], v[156:159], v[96:111]\n"
      "v_pk_mul_f16 v156, v191, v34 op_sel_hi:[0,1]\n"
      "v_pk_mul_f16 v157, v191, v35 op_sel_hi:[0,1]\n"
      "v_pk_mul_f16 v158, v191, v36 op_sel_hi:[0,1]\n"
      "v_pk_mul_f16 v159, v191, v37 op_sel_hi:[0,1]\n"
      "v_fma_mixlo_f16 v228, v71, v248, 0\n"
      "s_waitcnt lgkmcnt(1)\n"
      "v_mfma_f32_32x32x16_f16 v[80:95], v[148:151], v[160:163], v[80:95]\n"
      "ds_read_b128 v[144:147], v234 offset:31104\n"
      "v_pk_mul_f16 v160, v178, v22 op_sel_hi:[0,1]\n"
      "v_pk_mul_f16 v161, v178, v23 op_sel_hi:[0,1]\n"
      "v_pk_mul_f16 v162, v178, v24 op_sel_hi:[0,1]\n"
      "v_pk_mul_f16 v163, v178, v25 op_sel_hi:[0,1]\n"
      "v_fma_mixlo_f16 v216, v56, v244, 0\n"
      "v_mfma_f32_32x32x16_f16 v[96:111], v[148:151], v[156:159], v[96:111]\n"
      "v_pk_mul_f16 v156, v191, v38 op_sel_hi:[0,1]\n"
      "v_pk_mul_f16 v157, v191, v39 op_sel_hi:[0,1]\n"
      "v_pk_mul_f16 v158, v191, v40 op_sel_hi:[0,1]\n"
      "v_pk_mul_f16 v159, v191, v41 op_sel_hi:[0,1]\n"
      "v_fma_mixlo_f16 v229, v72, v248, 0\n"
      "s_waitcnt lgkmcnt(1)\n"
      "v_mfma_f32_32x32x16_f16 v[80:95], v[152:155], v[160:163], v[80:95]\n"
      "ds_read_b128 v[148:151], v234 offset:31968\n"
      "v_pk_mul_f16 v160, v178, v26 op_sel_hi:[0,1]\n"
      "v_pk_mul_f16 v161, v178, v27 op_sel_hi:[0,1]\n"
      "v_pk_mul_f16 v162, v178, v28 op_sel_hi:[0,1]\n"
      "v_pk_mul_f16 v163, v179, v16 op_sel_hi:[0,1]\n"
      "v_fma_mixlo_f16 v217, v57, v244, 0\n"
      "v_mfma_f32_32x32x16_f16 v[96:111], v[152:155], v[156:159], v[96:111]\n"
      "v_pk_mul_f16 v156, v191, v42 op_sel_hi:[0,1]\n"
      "v_pk_mul_f16 v157, v191, v43 op_sel_hi:[0,1]\n"
      "v_pk_mul_f16 v158, v191, v44 op_sel_hi:[0,1]\n"
      "v_pk_mul_f16 v159, v192, v32 op_sel_hi:[0,1]\n"
      "v_fma_mixlo_f16 v230, v73, v248, 0\n"
      "s_waitcnt lgkmcnt(1)\n"
      "v_mfma_f32_32x32x16_f16 v[80:95], v[144:147], v[160:163], v[80:95]\n"
      "ds_read_b128 v[152:155], v234 offset:32832\n"
      "v_pk_mul_f16 v160, v179, v17 op_sel_hi:[0,1]\n"
      "v_pk_mul_f16 v161, v179, v18 op_sel_hi:[0,1]\n"
      "v_pk_mul_f16 v162, v179, v19 op_sel_hi:[0,1]\n"
      "v_pk_mul_f16 v163, v179, v20 op_sel_hi:[0,1]\n"
      "v_fma_mixlo_f16 v218, v58, v244, 0\n"
      "v_mfma_f32_32x32x16_f16 v[96:111], v[144:147], v[156:159], v[96:111]\n"
      "v_pk_mul_f16 v156, v192, v33 op_sel_hi:[0,1]\n"
      "v_pk_mul_f16 v157, v192, v34 op_sel_hi:[0,1]\n"
      "v_pk_mul_f16 v158, v192, v35 op_sel_hi:[0,1]\n"
      "v_pk_mul_f16 v159, v192, v36 op_sel_hi:[0,1]\n"
      "v_fma_mixlo_f16 v231, v74, v248, 0\n"
      "s_waitcnt lgkmcnt(1)\n"
      "v_mfma_f32_32x32x16_f16 v[80:95], v[148:151], v[160:163], v[80:95]\n"
      "ds_read_b128 v[144:147], v234 offset:33696\n"
      "v_pk_mul_f16 v160, v179, v21 op_sel_hi:[0,1]\n"
      "v_pk_mul_f16 v161, v179, v22 op_sel_hi:[0,1]\n"
      "v_pk_mul_f16 v162, v179, v23 op_sel_hi:[0,1]\n"
      "v_pk_mul_f16 v163, v179, v24 op_sel_hi:[0,1]\n"
      "v_fma_mixlo_f16 v219, v59, v244, 0\n"
      "v_mfma_f32_32x32x16_f16 v[96:111], v[148:151], v[156:159], v[96:111]\n"
      "v_pk_mul_f16 v156, v192, v37 op_sel_hi:[0,1]\n"
      "v_pk_mul_f16 v157, v192, v38 op_sel_hi:[0,1]\n"
      "v_pk_mul_f16 v158, v192, v39 op_sel_hi:[0,1]\n"
      "v_pk_mul_f16 v159, v192, v40 op_sel_hi:[0,1]\n"
      "v_fma_mixlo_f16 v232, v75, v248, 0\n"
      "s_waitcnt lgkmcnt(1)\n"
      "v_mfma_f32_32x32x16_f16 v[80:95], v[152:155], v[160:163], v[80:95]\n"
      "ds_read_b128 v[148:151], v234 offset:34560\n"
      "v_pk_mul_f16 v160, v179, v25 op_sel_hi:[0,1]\n"
      "v_pk_mul_f16 v161, v179, v26 op_sel_hi:[0,1]\n"
      "v_pk_mul_f16 v162, v179, v27 op_sel_hi:[0,1]\n"
      "v_pk_mul_f16 v163, v179, v28 op_sel_hi:[0,1]\n"
      "v_fma_mixlo_f16 v220, v60, v244, 0\n"
      "v_mfma_f32_32x32x16_f16 v[96:111], v[152:155], v[156:159], v[96:111]\n"
      "v_pk_mul_f16 v156, v192, v41 op_sel_hi:[0,1]\n"
      "v_pk_mul_f16 v157, v192, v42 op_sel_hi:[0,1]\n"
      "v_pk_mul_f16 v158, v192, v43 op_sel_hi:[0,1]\n"
      "v_pk_mul_f16 v159, v192, v44 op_sel_hi:[0,1]\n"
      "v_fma_mixlo_f16 v233, v76, v248, 0\n"
      "s_waitcnt lgkmcnt(1)\n"
      "v_mfma_f32_32x32x16_f16 v[80:95], v[144:147], v[160:163], v[80:95]\n"
      "ds_read_b128 v[152:155], v234 offset:35424\n"
      "v_pk_mul_f16 v160, v180, v16 op_sel_hi:[0,1]\n"
      "v_pk_mul_f16 v161, v180, v17 op_sel_hi:[0,1]\n"
      "v_pk_mul_f16 v162, v180, v18 op_sel_hi:[0,1]\n"
      "v_pk_mul_f16 v163, v180, v19 op_sel_hi:[0,1]\n"
      "v_add3_u32 v236, v236, v242, v29\n"
      "v_mfma_f32_32x32x16_f16 v[96:111], v[144:147], v[156:159], v[96:111]\n"
      "v_pk_mul_f16 v156, v193, v32 op_sel_hi:[0,1]\n"
      "v_pk_mul_f16 v157, v193, v33 op_sel_hi:[0,1]\n"
      "v_pk_mul_f16 v158, v193, v34 op_sel_hi:[0,1]\n"
      "v_pk_mul_f16 v159, v193, v35 op_sel_hi:[0,1]\n"
      "v_add3_u32 v237, v237, v246, v45\n"
      "s_waitcnt lgkmcnt(1)\n"
      "v_mfma_f32_32x32x16_f16 v[80:95], v[148:151], v[160:163], v[80:95]\n"
      "ds_read_b128 v[144:147], v234 offset:36288\n"
      "v_pk_mul_f16 v160, v180, v20 op_sel_hi:[0,1]\n"
      "v_pk_mul_f16 v161, v180, v21 op_sel_hi:[0,1]\n"
      "v_pk_mul_f16 v162, v180, v22 op_sel_hi:[0,1]\n"
      "v_pk_mul_f16 v163, v180, v23 op_sel_hi:[0,1]\n"
      "v_mfma_f32_32x32x16_f16 v[96:111], v[148:151], v[156:159], v[96:111]\n"
      "v_pk_mul_f16 v156, v193, v36 op_sel_hi:[0,1]\n"
      "v_pk_mul_f16 v157, v193, v37 op_sel_hi:[0,1]\n"
      "v_pk_mul_f16 v158, v193, v38 op_sel_hi:[0,1]\n"
      "v_pk_mul_f16 v159, v193, v39 op_sel_hi:[0,1]\n"
      "s_waitcnt lgkmcnt(1)\n"
      "v_mfma_f32_32x32x16_f16 v[80:95], v[152:155], v[160:163], v[80:95]\n"
      "ds_read_b128 v[148:151], v234 offset:37152\n"
      "v_pk_mul_f16 v160, v180, v24 op_sel_hi:[0,1]\n"
      "v_pk_mul_f16 v161, v180, v25 op_sel_hi:[0,1]\n"
      "v_pk_mul_f16 v162, v180, v26 op_sel_hi:[0,1]\n"
      "v_pk_mul_f16 v163, v180, v27 op_sel_hi:[0,1]\n"
      "v_mfma_f32_32x32x16_f16 v[96:111], v[152:155], v[156:159], v[96:111]\n"
      "v_pk_mul_f16 v156, v193, v40 op_sel_hi:[0,1]\n"
      "v_pk_mul_f16 v157, v193, v41 op_sel_hi:[0,1]\n"
      "v_pk_mul_f16 v158, v193, v42 op_sel_hi:[0,1]\n"
      "v_pk_mul_f16 v159, v193, v43 op_sel_hi:[0,1]\n"
      "s_waitcnt lgkmcnt(1)\n"
      "v_mfma_f32_32x32x16_f16 v[80:95], v[144:147], v[160:163], v[80:95]\n"
      "ds_read_b128 v[152:155], v234 offset:38016\n"
      "v_pk_mul_f16 v160, v180, v28 op_sel_hi:[0,1]\n"
      "v_pk_mul_f16 v161, v181, v16 op_sel_hi:[0,1]\n"
      "v_pk_mul_f16 v162, v181, v17 op_sel_hi:[0,1]\n"
      "v_pk_mul_f16 v163, v181, v18 op_sel_hi:[0,1]\n"
      "v_mfma_f32_32x32x16_f16 v[96:111], v[144:147], v[156:159], v[96:111]\n"
      "v_pk_mul_f16 v156, v193, v44 op_sel_hi:[0,1]\n"
      "v_pk_mul_f16 v157, v194, v32 op_sel_hi:[0,1]\n"
      "v_pk_mul_f16 v158, v194, v33 op_sel_hi:[0,1]\n"
      "v_pk_mul_f16 v159, v194, v34 op_sel_hi:[0,1]\n"
      "s_waitcnt lgkmcnt(1)\n"
      "v_mfma_f32_32x32x16_f16 v[80:95], v[148:151], v[160:163], v[80:95]\n"
      "ds_read_b128 v[144:147], v234 offset:38880\n"
      "v_pk_mul_f16 v160, v181, v19 op_sel_hi:[0,1]\n"
      "v_pk_mul_f16 v161, v181, v20 op_sel_hi:[0,1]\n"
      "v_pk_mul_f16 v162, v181, v21 op_sel_hi:[0,1]\n"
      "v_pk_mul_f16 v163, v181, v22 op_sel_hi:[0,1]\n"
      "v_mfma_f32_32x32x16_f16 v[96:111], v[148:151], v[156:159], v[96:111]\n"
      "v_pk_mul_f16 v156, v194, v35 op_sel_hi:[0,1]\n"
      "v_pk_mul_f16 v157, v194, v36 op_sel_hi:[0,1]\n"
      "v_pk_mul_f16 v158, v194, v37 op_sel_hi:[0,1]\n"
      "v_pk_mul_f16 v159, v194, v38 op_sel_hi:[0,1]\n"
      "s_waitcnt lgkmcnt(1)\n"
      "v_mfma_f32_32x32x16_f16 v[80:95], v[152:155], v[160:163], v[80:95]\n"
      "ds_read_b128 v[148:151], v234 offset:39744\n"
      "v_pk_mul_f16 v160, v181, v23 op_sel_hi:[0,1]\n"
      "v_pk_mul_f16 v161, v181, v24 op_sel_hi:[0,1]\n"
      "v_pk_mul_f16 v162, v181, v25 op_sel_hi:[0,1]\n"
      "v_pk_mul_f16 v163, v181, v26 op_sel_hi:[0,1]\n"
      "v_mfma_f32_32x32x16_f16 v[96:111], v[152:155], v[156:159], v[96:111]\n"
      "v_pk_mul_f16 v156, v194, v39 op_sel_hi:[0,1]\n"
      "v_pk_mul_f16 v157, v194, v40 op_sel_hi:[0,1]\n"
      "v_pk_mul_f16 v158, v194, v41 op_sel_hi:[0,1]\n"
      "v_pk_mul_f16 v159, v194, v42 op_sel_hi:[0,1]\n"
      "s_waitcnt lgkmcnt(1)\n"
      "v_mfma_f32_32x32x16_f16 v[80:95], v[144:147], v[160:163], v[80:95]\n"
      "ds_read_b128 v[152:155], v234 offset:40608\n"
      "v_pk_mul_f16 v160, v181, v27 op_sel_hi:[0,1]\n"
      "v_pk_mul_f16 v161, v181, v28 op_sel_hi:[0,1]\n"
      "v_pk_mul_f16 v162, v182, v16 op_sel_hi:[0,1]\n"
      "v_pk_mul_f16 v163, v182, v17 op_sel_hi:[0,1]\n"
      "v_mfma_f32_32x32x16_f16 v[96:111], v[144:147], v[156:159], v[96:111]\n"
      "v_pk_mul_f16 v156, v194, v43 op_sel_hi:[0,1]\n"
      "v_pk_mul_f16 v157, v194, v44 op_sel_hi:[0,1]\n"
      "v_pk_mul_f16 v158, v195, v32 op_sel_hi:[0,1]\n"
      "v_pk_mul_f16 v159, v195, v33 op_sel_hi:[0,1]\n"
      "s_waitcnt lgkmcnt(1)\n"
      "v_mfma_f32_32x32x16_f16 v[80:95], v[148:151], v[160:163], v[80:95]\n"
      "ds_read_b128 v[144:147], v234 offset:41472\n"
      "v_pk_mul_f16 v160, v182, v18 op_sel_hi:[0,1]\n"
      "v_pk_mul_f16 v161, v182, v19 op_sel_hi:[0,1]\n"
      "v_pk_mul_f16 v162, v182, v20 op_sel_hi:[0,1]\n"
      "v_pk_mul_f16 v163, v182, v21 op_sel_hi:[0,1]\n"
      "v_mfma_f32_32x32x16_f16 v[96:111], v[148:151], v[156:159], v[96:111]\n"
      "v_pk_mul_f16 v156, v195, v34 op_sel_hi:[0,1]\n"
      "v_pk_mul_f16 v157, v195, v35 op_sel_hi:[0,1]\n"
      "v_pk_mul_f16 v158, v195, v36 op_sel_hi:[0,1]\n"
      "v_pk_mul_f16 v159, v195, v37 op_sel_hi:[0,1]\n"
      "s_waitcnt lgkmcnt(1)\n"
      "v_mfma_f32_32x32x16_f16 v[80:95], v[152:155], v[160:163], v[80:95]\n"
      "ds_read_b128 v[148:151], v234 offset:42336\n"
      "v_pk_mul_f16 v160, v182, v22 op_sel_hi:[0,1]\n"
      "v_pk_mul_f16 v161, v182, v23 op_sel_hi:[0,1]\n"
      "v_pk_mul_f16 v162, v182, v24 op_sel_hi:[0,1]\n"
      "v_pk_mul_f16 v163, v182, v25 op_sel_hi:[0,1]\n"
      "v_mfma_f32_32x32x16_f16 v[96:111], v[152:155], v[156:159], v[96:111]\n"
      "v_pk_mul_f16 v156, v195, v38 op_sel_hi:[0,1]\n"
      "v_pk_mul_f16 v157, v195, v39 op_sel_hi:[0,1]\n"
      "v_pk_mul_f16 v158, v195, v40 op_sel_hi:[0,1]\n"
      "v_pk_mul_f16 v159, v195, v41 op_sel_hi:[0,1]\n"
      "s_waitcnt lgkmcnt(1)\n"
      "v_mfma_f32_32x32x16_f16 v[80:95], v[144:147], v[160:163], v[80:95]\n"
      "ds_read_b128 v[152:155], v234 offset:43200\n"
      "v_pk_mul_f16 v160, v182, v26 op_sel_hi:[0,1]\n"
      "v_pk_mul_f16 v161, v182, v27 op_sel_hi:[0,1]\n"
      "v_pk_mul_f16 v162, v182, v28 op_sel_hi:[0,1]\n"
      "v_pk_mul_f16 v163, v183, v16 op_sel_hi:[0,1]\n"
      "v_mfma_f32_32x32x16_f16 v[96:111], v[144:147], v[156:159], v[96:111]\n"
      "v_pk_mul_f16 v156, v195, v42 op_sel_hi:[0,1]\n"
      "v_pk_mul_f16 v157, v195, v43 op_sel_hi:[0,1]\n"
      "v_pk_mul_f16 v158, v195, v44 op_sel_hi:[0,1]\n"
      "v_pk_mul_f16 v159, v196, v32 op_sel_hi:[0,1]\n"
      "s_waitcnt lgkmcnt(1)\n"
      "v_mfma_f32_32x32x16_f16 v[80:95], v[148:151], v[160:163], v[80:95]\n"
      "ds_read_b128 v[144:147], v234 offset:44064\n"
      "v_pk_mul_f16 v160, v183, v17 op_sel_hi:[0,1]\n"
      "v_pk_mul_f16 v161, v183, v18 op_sel_hi:[0,1]\n"
      "v_pk_mul_f16 v162, v183, v19 op_sel_hi:[0,1]\n"
      "v_pk_mul_f16 v163, v183, v20 op_sel_hi:[0,1]\n"
      "v_mfma_f32_32x32x16_f16 v[96:111], v[148:151], v[156:159], v[96:111]\n"
      "v_pk_mul_f16 v156, v196, v33 op_sel_hi:[0,1]\n"
      "v_pk_mul_f16 v157, v196, v34 op_sel_hi:[0,1]\n"
      "v_pk_mul_f16 v158, v196, v35 op_sel_hi:[0,1]\n"
      "v_pk_mul_f16 v159, v196, v36 op_sel_hi:[0,1]\n"
      "s_waitcnt lgkmcnt(1)\n"
      "v_mfma_f32_32x32x16_f16 v[80:95], v[152:155], v[160:163], v[80:95]\n"
      "ds_read_b128 v[148:151], v234 offset:44928\n"
      "v_pk_mul_f16 v160, v183, v21 op_sel_hi:[0,1]\n"
      "v_pk_mul_f16 v161, v183, v22 op_sel_hi:[0,1]\n"
      "v_pk_mul_f16 v162, v183, v23 op_sel_hi:[0,1]\n"
      "v_pk_mul_f16 v163, v183, v24 op_sel_hi:[0,1]\n"
      "v_mfma_f32_32x32x16_f16 v[96:111], v[152:155], v[156:159], v[96:111]\n"
      "v_pk_mul_f16 v156, v196, v37 op_sel_hi:[0,1]\n"
      "v_pk_mul_f16 v157, v196, v38 op_sel_hi:[0,1]\n"
      "v_pk_mul_f16 v158, v196, v39 op_sel_hi:[0,1]\n"
      "v_pk_mul_f16 v159, v196, v40 op_sel_hi:[0,1]\n"
      "s_waitcnt lgkmcnt(1)\n"
      "v_mfma_f32_32x32x16_f16 v[80:95], v[144:147], v[160:163], v[80:95]\n"
      "ds_read_b128 v[152:155], v234 offset:45792\n"
      "v_pk_mul_f16 v160, v183, v25 op_sel_hi:[0,1]\n"
      "v_pk_mul_f16 v161, v183, v26 op_sel_hi:[0,1]\n"
      "v_pk_mul_f16 v162, v183, v27 op_sel_hi:[0,1]\n"
      "v_pk_mul_f16 v163, v183, v28 op_sel_hi:[0,1]\n"
      "v_mfma_f32_32x32x16_f16 v[96:111], v[144:147], v[156:159], v[96:111]\n"
      "v_pk_mul_f16 v156, v196, v41 op_sel_hi:[0,1]\n"
      "v_pk_mul_f16 v157, v196, v42 op_sel_hi:[0,1]\n"
      "v_pk_mul_f16 v158, v196, v43 op_sel_hi:[0,1]\n"
      "v_pk_mul_f16 v159, v196, v44 op_sel_hi:[0,1]\n"
      "s_waitcnt lgkmcnt(1)\n"
      "v_mfma_f32_32x32x16_f16 v[80:95], v[148:151], v[160:163], v[80:95]\n"
      "ds_read_b128 v[144:147], v234 offset:46656\n"
      "v_pk_mul_f16 v160, v184, v16 op_sel_hi:[0,1]\n"
      "v_pk_mul_f16 v161, v184, v17 op_sel_hi:[0,1]\n"
      "v_pk_mul_f16 v162, v184, v18 op_sel_hi:[0,1]\n"
      "v_pk_mul_f16 v163, v184, v19 op_sel_hi:[0,1]\n"
      "v_mfma_f32_32x32x16_f16 v[96:111], v[148:151], v[156:159], v[96:111]\n"
      "v_pk_mul_f16 v156, v197, v32 op_sel_hi:[0,1]\n"
      "v_pk_mul_f16 v157, v197, v33 op_sel_hi:[0,1]\n"
      "v_pk_mul_f16 v158, v197, v34 op_sel_hi:[0,1]\n"
      "v_pk_mul_f16 v159, v197, v35 op_sel_hi:[0,1]\n"
      "s_waitcnt lgkmcnt(1)\n"
      "v_mfma_f32_32x32x16_f16 v[80:95], v[152:155], v[160:163], v[80:95]\n"
      "ds_read_b128 v[148:151], v234 offset:47520\n"
      "v_pk_mul_f16 v160, v184, v20 op_sel_hi:[0,1]\n"
      "v_pk_mul_f16 v161, v184, v21 op_sel_hi:[0,1]\n"
      "v_pk_mul_f16 v162, v184, v22 op_sel_hi:[0,1]\n"
      "v_pk_mul_f16 v163, v184, v23 op_sel_hi:[0,1]\n"
      "v_mfma_f32_32x32x16_f16 v[96:111], v[152:155], v[156:159], v[96:111]\n"
      "v_pk_mul_f16 v156, v197, v36 op_sel_hi:[0,1]\n"
      "v_pk_mul_f16 v157, v197, v37 op_sel_hi:[0,1]\n"
      "v_pk_mul_f16 v158, v197, v38 op_sel_hi:[0,1]\n"
      "v_pk_mul_f16 v159, v197, v39 op_sel_hi:[0,1]\n"
      "s_waitcnt lgkmcnt(1)\n"
      "v_mfma_f32_32x32x16_f16 v[80:95], v[144:147], v[160:163], v[80:95]\n"
      "ds_read_b128 v[152:155], v234 offset:48384\n"
      "v_pk_mul_f16 v160, v184, v24 op_sel_hi:[0,1]\n"
      "v_pk_mul_f16 v161, v184, v25 op_sel_hi:[0,1]\n"
      "v_pk_mul_f16 v162, v184, v26 op_sel_hi:[0,1]\n"
      "v_pk_mul_f16 v163, v184, v27 op_sel_hi:[0,1]\n"
      "v_mfma_f32_32x32x16_f16 v[96:111], v[144:147], v[156:159], v[96:111]\n"
      "v_pk_mul_f16 v156, v197, v40 op_sel_hi:[0,1]\n"
      "v_pk_mul_f16 v157, v197, v41 op_sel_hi:[0,1]\n"
      "v_pk_mul_f16 v158, v197, v42 op_sel_hi:[0,1]\n"
      "v_pk_mul_f16 v159, v197, v43 op_sel_hi:[0,1]\n"
      "s_waitcnt lgkmcnt(1)\n"
      "v_mfma_f32_32x32x16_f16 v[80:95], v[148:151], v[160:163], v[80:95]\n"
      "s_barrier\n"
      "ds_read_b128 v[144:147], v234 offset:49248\n"
      "v_pk_mul_f16 v160, v184, v28 op_sel_hi:[0,1]\n"
      "v_pk_mul_f16 v164, v197, v44 op_sel_hi:[0,1]\n"
      "v_pk_mul_f16 v168, v208, v16 op_sel_hi:[0,1]\n"
      "v_pk_mul_f16 v169, v208, v17 op_sel_hi:[0,1]\n"
      "v_mfma_f32_32x32x16_f16 v[96:111], v[148:151], v[156:159], v[96:111]\n"
      "v_pk_mul_f16 v170, v208, v18 op_sel_hi:[0,1]\n"
      "v_pk_mul_f16 v171, v208, v19 op_sel_hi:[0,1]\n"
      "v_pk_mul_f16 v156, v221, v32 op_sel_hi:[0,1]\n"
      "v_pk_mul_f16 v157, v221, v33 op_sel_hi:[0,1]\n"
      "s_waitcnt lgkmcnt(1)\n"
      "v_mfma_f32_32x32x16_f16 v[80:95], v[152:155], v[160:163], v[80:95]\n"
      "ds_read_b128 v[148:151], v234 offset:50112\n"
      "v_pk_mul_f16 v158, v221, v34 op_sel_hi:[0,1]\n"
      "v_pk_mul_f16 v159, v221, v35 op_sel_hi:[0,1]\n"
      "v_pk_mul_f16 v160, v208, v20 op_sel_hi:[0,1]\n"
      "v_pk_mul_f16 v161, v208, v21 op_sel_hi:[0,1]\n"
      "v_mfma_f32_32x32x16_f16 v[96:111], v[152:155], v[164:167], v[96:111]\n"
      "v_pk_mul_f16 v162, v208, v22 op_sel_hi:[0,1]\n"
      "v_pk_mul_f16 v163, v208, v23 op_sel_hi:[0,1]\n"
      "v_pk_mul_f16 v164, v221, v36 op_sel_hi:[0,1]\n"
      "v_pk_mul_f16 v165, v221, v37 op_sel_hi:[0,1]\n"
      "s_waitcnt lgkmcnt(1)\n"
      "v_mfma_f32_32x32x16_f16 v[48:63], v[144:147], v[168:171], 0\n"
      "ds_read_b128 v[152:155], v234 offset:50976\n"
      "v_pk_mul_f16 v166, v221, v38 op_sel_hi:[0,1]\n"
      "v_pk_mul_f16 v167, v221, v39 op_sel_hi:[0,1]\n"
      "v_pk_mul_f16 v168, v208, v24 op_sel_hi:[0,1]\n"
      "v_pk_mul_f16 v169, v208, v25 op_sel_hi:[0,1]\n"
      "v_mfma_f32_32x32x16_f16 v[64:79], v[144:147], v[156:159], 0\n"
      "v_pk_mul_f16 v170, v208, v26 op_sel_hi:[0,1]\n"
      "v_pk_mul_f16 v171, v208, v27 op_sel_hi:[0,1]\n"
      "v_pk_mul_f16 v156, v221, v40 op_sel_hi:[0,1]\n"
      "v_pk_mul_f16 v157, v221, v41 op_sel_hi:[0,1]\n"
      "s_waitcnt lgkmcnt(1)\n"
      "v_mfma_f32_32x32x16_f16 v[48:63], v[148:151], v[160:163], v[48:63]\n"
      "ds_read_b128 v[144:147], v234 offset:51840\n"
      "v_pk_mul_f16 v158, v221, v42 op_sel_hi:[0,1]\n"
      "v_pk_mul_f16 v159, v221, v43 op_sel_hi:[0,1]\n"
      "v_pk_mul_f16 v160, v208, v28 op_sel_hi:[0,1]\n"
      "v_pk_mul_f16 v161, v209, v16 op_sel_hi:[0,1]\n"
      "v_mfma_f32_32x32x16_f16 v[64:79], v[148:151], v[164:167], v[64:79]\n"
      "v_pk_mul_f16 v162, v209, v17 op_sel_hi:[0,1]\n"
      "v_pk_mul_f16 v163, v209, v18 op_sel_hi:[0,1]\n"
      "v_pk_mul_f16 v164, v221, v44 op_sel_hi:[0,1]\n"
      "v_pk_mul_f16 v165, v222, v32 op_sel_hi:[0,1]\n"
      "s_waitcnt lgkmcnt(1)\n"
      "v_mfma_f32_32x32x16_f16 v[48:63], v[152:155], v[168:171], v[48:63]\n"
      "ds_read_b128 v[148:151], v234 offset:52704\n"
      "v_pk_mul_f16 v166, v222, v33 op_sel_hi:[0,1]\n"
      "v_pk_mul_f16 v167, v222, v34 op_sel_hi:[0,1]\n"
      "v_add3_u32 v238, v238, v236, s85\n"
      "v_add_u32 v245, s84, v236\n"
      "v_add3_u32 v239, v239, v237, s85\n"
      "v_mfma_f32_32x32x16_f16 v[64:79], v[152:155], v[156:159], v[64:79]\n"
      "v_add_u32 v249, s84, v237\n"
      "ds_read_b32 v204, v15\n"
      "v_lshrrev_b32 v251, 1, v15\n"
      "v_pk_mul_f16 v156, v209, v19 op_sel_hi:[0,1]\n"
      "s_waitcnt lgkmcnt(2)\n"
      "v_mfma_f32_32x32x16_f16 v[48:63], v[144:147], v[160:163], v[48:63]\n"
      "ds_read_b128 v[152:155], v234 offset:53568\n"
      "v_pk_mul_f16 v157, v209, v20 op_sel_hi:[0,1]\n"
      "v_pk_mul_f16 v158, v209, v21 op_sel_hi:[0,1]\n"
      "v_pk_mul_f16 v159, v209, v22 op_sel_hi:[0,1]\n"
      "v_pk_mul_f16 v160, v222, v35 op_sel_hi:[0,1]\n"
      "v_mfma_f32_32x32x16_f16 v[64:79], v[144:147], v[164:167], v[64:79]\n"
      "v_pk_mul_f16 v161, v222, v36 op_sel_hi:[0,1]\n"
      "v_pk_mul_f16 v162, v222, v37 op_sel_hi:[0,1]\n"
      "v_pk_mul_f16 v163, v222, v38 op_sel_hi:[0,1]\n"
      "v_pk_mul_f16 v164, v209, v23 op_sel_hi:[0,1]\n"
      "s_waitcnt lgkmcnt(2)\n"
      "v_mfma_f32_32x32x16_f16 v[48:63], v[148:151], v[156:159], v[48:63]\n"
      "ds_read_b128 v[144:147], v234 offset:54432\n"
      "v_pk_mul_f16 v165, v209, v24 op_sel_hi:[0,1]\n"
      "v_pk_mul_f16 v166, v209, v25 op_sel_hi:[0,1]\n"
      "v_pk_mul_f16 v167, v209, v26 op_sel_hi:[0,1]\n"
      "v_pk_mul_f16 v156, v222, v39 op_sel_hi:[0,1]\n"
      "v_mfma_f32_32x32x16_f16 v[64:79], v[148:151], v[160:163], v[64:79]\n"
      "v_pk_mul_f16 v157, v222, v40 op_sel_hi:[0,1]\n"
      "v_pk_mul_f16 v158, v222, v41 op_sel_hi:[0,1]\n"
      "v_pk_mul_f16 v159, v222, v42 op_sel_hi:[0,1]\n"
      "v_pk_mul_f16 v160, v209, v27 op_sel_hi:[0,1]\n"
      "s_waitcnt lgkmcnt(1)\n"
      "v_mfma_f32_32x32x16_f16 v[48:63], v[152:155], v[164:167], v[48:63]\n"
      "ds_read_b128 v[148:151], v234 offset:55296\n"
      "v_pk_mul_f16 v161, v209, v28 op_sel_hi:[0,1]\n"
      "v_pk_mul_f16 v162, v210, v16 op_sel_hi:[0,1]\n"
      "v_pk_mul_f16 v163, v210, v17 op_sel_hi:[0,1]\n"
      "v_pk_mul_f16 v164, v222, v43 op_sel_hi:[0,1]\n"
      "v_mfma_f32_32x32x16_f16 v[64:79], v[152:155], v[156:159], v[64:79]\n"
      "v_pk_mul_f16 v165, v222, v44 op_sel_hi:[0,1]\n"
      "v_pk_mul_f16 v166, v223, v32 op_sel_hi:[0,1]\n"
      "v_pk_mul_f16 v167, v223, v33 op_sel_hi:[0,1]\n"
      "v_pk_mul_f16 v156, v210, v18 op_sel_hi:[0,1]\n"
      "s_waitcnt lgkmcnt(1)\n"
      "v_mfma_f32_32x32x16_f16 v[48:63], v[144:147], v[160:163], v[48:63]\n"
      "ds_read_b128 v[152:155], v234 offset:56160\n"
      "v_pk_mul_f16 v157, v210, v19 op_sel_hi:[0,1]\n"
      "v_pk_mul_f16 v158, v210, v20 op_sel_hi:[0,1]\n"
      "v_pk_mul_f16 v159, v210, v21 op_sel_hi:[0,1]\n"
      "v_pk_mul_f16 v160, v223, v34 op_sel_hi:[0,1]\n"
      "v_mfma_f32_32x32x16_f16 v[64:79], v[144:147], v[164:167], v[64:79]\n"
      "v_pk_mul_f16 v161, v223, v35 op_sel_hi:[0,1]\n"
      "v_pk_mul_f16 v162, v223, v36 op_sel_hi:[0,1]\n"
      "v_pk_mul_f16 v163, v223, v37 op_sel_hi:[0,1]\n"
      "v_pk_mul_f16 v164, v210, v22 op_sel_hi:[0,1]\n"
      "s_waitcnt lgkmcnt(1)\n"
      "v_mfma_f32_32x32x16_f16 v[48:63], v[148:151], v[156:159], v[48:63]\n"
      "ds_read_b128 v[144:147], v234 offset:57024\n"
      "v_pk_mul_f16 v165, v210, v23 op_sel_hi:[0,1]\n"
      "v_pk_mul_f16 v166, v210, v24 op_sel_hi:[0,1]\n"
      "v_pk_mul_f16 v167, v210, v25 op_sel_hi:[0,1]\n"
      "v_pk_mul_f16 v156, v223, v38 op_sel_hi:[0,1]\n"
      "v_mfma_f32_32x32x16_f16 v[64:79], v[148:151], v[160:163], v[64:79]\n"
      "v_pk_mul_f16 v157, v223, v39 op_sel_hi:[0,1]\n"
      "v_pk_mul_f16 v158, v223, v40 op_sel_hi:[0,1]\n"
      "v_pk_mul_f16 v159, v223, v41 op_sel_hi:[0,1]\n"
      "v_pk_mul_f16 v160, v210, v26 op_sel_hi:[0,1]\n"
      "s_waitcnt lgkmcnt(1)\n"
      "v_mfma_f32_32x32x16_f16 v[48:63], v[152:155], v[164:167], v[48:63]\n"
      "ds_read_b128 v[148:151], v234 offset:57888\n"
      "v_pk_mul_f16 v161, v210, v27 op_sel_hi:[0,1]\n"
      "v_pk_mul_f16 v162, v210, v28 op_sel_hi:[0,1]\n"
      "v_pk_mul_f16 v163, v211, v16 op_sel_hi:[0,1]\n"
      "v_pk_mul_f16 v164, v223, v42 op_sel_hi:[0,1]\n"
      "v_mfma_f32_32x32x16_f16 v[64:79], v[152:155], v[156:159], v[64:79]\n"
      "v_pk_mul_f16 v165, v223, v43 op_sel_hi:[0,1]\n"
      "v_pk_mul_f16 v166, v223, v44 op_sel_hi:[0,1]\n"
      "v_pk_mul_f16 v167, v224, v32 op_sel_hi:[0,1]\n"
      "v_pk_mul_f16 v156, v211, v17 op_sel_hi:[0,1]\n"
      "s_waitcnt lgkmcnt(1)\n"
      "v_mfma_f32_32x32x16_f16 v[48:63], v[144:147], v[160:163], v[48:63]\n"
      "ds_read_b128 v[152:155], v234 offset:58752\n"
      "v_pk_mul_f16 v157, v211, v18 op_sel_hi:[0,1]\n"
      "v_pk_mul_f16 v158, v211, v19 op_sel_hi:[0,1]\n"
      "v_pk_mul_f16 v159, v211, v20 op_sel_hi:[0,1]\n"
      "v_pk_mul_f16 v160, v224, v33 op_sel_hi:[0,1]\n"
      "v_mfma_f32_32x32x16_f16 v[64:79], v[144:147], v[164:167], v[64:79]\n"
      "v_pk_mul_f16 v161, v224, v34 op_sel_hi:[0,1]\n"
      "v_pk_mul_f16 v162, v224, v35 op_sel_hi:[0,1]\n"
      "v_pk_mul_f16 v163, v224, v36 op_sel_hi:[0,1]\n"
      "v_pk_mul_f16 v164, v211, v21 op_sel_hi:[0,1]\n"
      "s_waitcnt lgkmcnt(1)\n"
      "v_mfma_f32_32x32x16_f16 v[48:63], v[148:151], v[156:159], v[48:63]\n"
      "ds_read_b128 v[144:147], v234 offset:59616\n"
      "v_pk_mul_f16 v165, v211, v22 op_sel_hi:[0,1]\n"
      "v_pk_mul_f16 v166, v211, v23 op_sel_hi:[0,1]\n"
      "v_pk_mul_f16 v167, v211, v24 op_sel_hi:[0,1]\n"
      "v_pk_mul_f16 v156, v224, v37 op_sel_hi:[0,1]\n"
      "v_mfma_f32_32x32x16_f16 v[64:79], v[148:151], v[160:163], v[64:79]\n"
      "v_pk_mul_f16 v157, v224, v38 op_sel_hi:[0,1]\n"
      "v_pk_mul_f16 v158, v224, v39 op_sel_hi:[0,1]\n"
      "v_pk_mul_f16 v159, v224, v40 op_sel_hi:[0,1]\n"
      "v_pk_mul_f16 v160, v211, v25 op_sel_hi:[0,1]\n"
      "s_waitcnt lgkmcnt(1)\n"
      "v_mfma_f32_32x32x16_f16 v[48:63], v[152:155], v[164:167], v[48:63]\n"
      "ds_read_b128 v[148:151], v234 offset:60480\n"
      "v_pk_mul_f16 v161, v211, v26 op_sel_hi:[0,1]\n"
      "v_pk_mul_f16 v162, v211, v27 op_sel_hi:[0,1]\n"
      "v_pk_mul_f16 v163, v211, v28 op_sel_hi:[0,1]\n"
      "v_pk_mul_f16 v164, v224, v41 op_sel_hi:[0,1]\n"
      "v_mfma_f32_32x32x16_f16 v[64:79], v[152:155], v[156:159], v[64:79]\n"
      "v_pk_mul_f16 v165, v224, v42 op_sel_hi:[0,1]\n"
      "v_pk_mul_f16 v166, v224, v43 op_sel_hi:[0,1]\n"
      "v_pk_mul_f16 v167, v224, v44 op_sel_hi:[0,1]\n"
      "v_pk_mul_f16 v156, v212, v16 op_sel_hi:[0,1]\n"
      "s_waitcnt lgkmcnt(1)\n"
      "v_mfma_f32_32x32x16_f16 v[48:63], v[144:147], v[160:163], v[48:63]\n"
      "ds_read_b128 v[152:155], v234 offset:61344\n"
      "v_pk_mul_f16 v157, v212, v17 op_sel_hi:[0,1]\n"
      "v_pk_mul_f16 v158, v212, v18 op_sel_hi:[0,1]\n"
      "v_pk_mul_f16 v159, v212, v19 op_sel_hi:[0,1]\n"
      "v_pk_mul_f16 v160, v225, v32 op_sel_hi:[0,1]\n"
      "v_mfma_f32_32x32x16_f16 v[64:79], v[144:147], v[164:167], v[64:79]\n"
      "v_pk_mul_f16 v161, v225, v33 op_sel_hi:[0,1]\n"
      "v_pk_mul_f16 v162, v225, v34 op_sel_hi:[0,1]\n"
      "v_pk_mul_f16 v163, v225, v35 op_sel_hi:[0,1]\n"
      "v_pk_mul_f16 v164, v212, v20 op_sel_hi:[0,1]\n"
      "s_waitcnt lgkmcnt(1)\n"
      "v_mfma_f32_32x32x16_f16 v[48:63], v[148:151], v[156:159], v[48:63]\n"
      "ds_read_b128 v[144:147], v234 offset:62208\n"
      "v_pk_mul_f16 v165, v212, v21 op_sel_hi:[0,1]\n"
      "v_pk_mul_f16 v166, v212, v22 op_sel_hi:[0,1]\n"
      "v_pk_mul_f16 v167, v212, v23 op_sel_hi:[0,1]\n"
      "v_pk_mul_f16 v156, v225, v36 op_sel_hi:[0,1]\n"
      "v_mfma_f32_32x32x16_f16 v[64:79], v[148:151], v[160:163], v[64:79]\n"
      "v_pk_mul_f16 v157, v225, v37 op_sel_hi:[0,1]\n"
      "v_pk_mul_f16 v158, v225, v38 op_sel_hi:[0,1]\n"
      "v_pk_mul_f16 v159, v225, v39 op_sel_hi:[0,1]\n"
      "v_pk_mul_f16 v160, v212, v24 op_sel_hi:[0,1]\n"
      "s_waitcnt lgkmcnt(1)\n"
      "v_mfma_f32_32x32x16_f16 v[48:63], v[152:155], v[164:167], v[48:63]\n"
      "ds_read_b128 v[148:151], v234 offset:63072\n"
      "v_pk_mul_f16 v161, v212, v25 op_sel_hi:[0,1]\n"
      "v_pk_mul_f16 v162, v212, v26 op_sel_hi:[0,1]\n"
      "v_pk_mul_f16 v163, v212, v27 op_sel_hi:[0,1]\n"
      "v_pk_mul_f16 v164, v225, v40 op_sel_hi:[0,1]\n"
      "v_mfma_f32_32x32x16_f16 v[64:79], v[152:155], v[156:159], v[64:79]\n"
      "v_pk_mul_f16 v165, v225, v41 op_sel_hi:[0,1]\n"
      "v_pk_mul_f16 v166, v225, v42 op_sel_hi:[0,1]\n"
      "v_pk_mul_f16 v167, v225, v43 op_sel_hi:[0,1]\n"
      "v_pk_mul_f16 v156, v212, v28 op_sel_hi:[0,1]\n"
      "s_waitcnt lgkmcnt(1)\n"
      "v_mfma_f32_32x32x16_f16 v[48:63], v[144:147], v[160:163], v[48:63]\n"
      "ds_read_b128 v[152:155], v234 offset:63936\n"
      "v_pk_mul_f16 v157, v213, v16 op_sel_hi:[0,1]\n"
      "v_pk_mul_f16 v158, v213, v17 op_sel_hi:[0,1]\n"
      "v_pk_mul_f16 v159, v213, v18 op_sel_hi:[0,1]\n"
      "v_pk_mul_f16 v160, v225, v44 op_sel_hi:[0,1]\n"
      "v_mfma_f32_32x32x16_f16 v[64:79], v[144:147], v[164:167], v[64:79]\n"
      "v_pk_mul_f16 v161, v226, v32 op_sel_hi:[0,1]\n"
      "v_pk_mul_f16 v162, v226, v33 op_sel_hi:[0,1]\n"
      "v_pk_mul_f16 v163, v226, v34 op_sel_hi:[0,1]\n"
      "v_pk_mul_f16 v164, v213, v19 op_sel_hi:[0,1]\n"
      "s_waitcnt lgkmcnt(1)\n"
      "v_mfma_f32_32x32x16_f16 v[48:63], v[148:151], v[156:159], v[48:63]\n"
      "ds_read_b128 v[144:147], v235 offset:0\n"
      "v_pk_mul_f16 v165, v213, v20 op_sel_hi:[0,1]\n"
      "v_pk_mul_f16 v166, v213, v21 op_sel_hi:[0,1]\n"
      "v_pk_mul_f16 v167, v213, v22 op_sel_hi:[0,1]\n"
      "v_pk_mul_f16 v156, v226, v35 op_sel_hi:[0,1]\n"
      "v_mfma_f32_32x32x16_f16 v[64:79], v[148:151], v[160:163], v[64:79]\n"
      "v_pk_mul_f16 v157, v226, v36 op_sel_hi:[0,1]\n"
      "v_pk_mul_f16 v158, v226, v37 op_sel_hi:[0,1]\n"
      "v_pk_mul_f16 v159, v226, v38 op_sel_hi:[0,1]\n"
      "v_pk_mul_f16 v160, v213, v23 op_sel_hi:[0,1]\n"
      "s_waitcnt lgkmcnt(1)\n"
      "v_mfma_f32_32x32x16_f16 v[48:63], v[152:155], v[164:167], v[48:63]\n"
      "ds_read_b128 v[148:151], v235 offset:864\n"
      "v_pk_mul_f16 v161, v213, v24 op_sel_hi:[0,1]\n"
      "v_pk_mul_f16 v162, v213, v25 op_sel_hi:[0,1]\n"
      "v_pk_mul_f16 v163, v213, v26 op_sel_hi:[0,1]\n"
      "v_pk_mul_f16 v164, v226, v39 op_sel_hi:[0,1]\n"
      "v_mfma_f32_32x32x16_f16 v[64:79], v[152:155], v[156:159], v[64:79]\n"
      "v_pk_mul_f16 v165, v226, v40 op_sel_hi:[0,1]\n"
      "v_pk_mul_f16 v166, v226, v41 op_sel_hi:[0,1]\n"
      "v_pk_mul_f16 v167, v226, v42 op_sel_hi:[0,1]\n"
      "v_pk_mul_f16 v156, v213, v27 op_sel_hi:[0,1]\n"
      "s_waitcnt lgkmcnt(1)\n"
      "v_mfma_f32_32x32x16_f16 v[48:63], v[144:147], v[160:163], v[48:63]\n"
      "ds_read_b128 v[152:155], v235 offset:1728\n"
      "v_pk_mul_f16 v157, v213, v28 op_sel_hi:[0,1]\n"
      "v_pk_mul_f16 v158, v214, v16 op_sel_hi:[0,1]\n"
      "v_pk_mul_f16 v159, v214, v17 op_sel_hi:[0,1]\n"
      "v_pk_mul_f16 v160, v226, v43 op_sel_hi:[0,1]\n"
      "v_mfma_f32_32x32x16_f16 v[64:79], v[144:147], v[164:167], v[64:79]\n"
      "v_pk_mul_f16 v161, v226, v44 op_sel_hi:[0,1]\n"
      "v_pk_mul_f16 v162, v227, v32 op_sel_hi:[0,1]\n"
      "v_pk_mul_f16 v163, v227, v33 op_sel_hi:[0,1]\n"
      "v_pk_mul_f16 v164, v214, v18 op_sel_hi:[0,1]\n"
      "s_waitcnt lgkmcnt(1)\n"
      "v_mfma_f32_32x32x16_f16 v[48:63], v[148:151], v[156:159], v[48:63]\n"
      "ds_read_b128 v[144:147], v235 offset:2592\n"
      "v_pk_mul_f16 v165, v214, v19 op_sel_hi:[0,1]\n"
      "v_pk_mul_f16 v166, v214, v20 op_sel_hi:[0,1]\n"
      "v_pk_mul_f16 v167, v214, v21 op_sel_hi:[0,1]\n"
      "v_pk_mul_f16 v156, v227, v34 op_sel_hi:[0,1]\n"
      "v_mfma_f32_32x32x16_f16 v[64:79], v[148:151], v[160:163], v[64:79]\n"
      "v_pk_mul_f16 v157, v227, v35 op_sel_hi:[0,1]\n"
      "v_pk_mul_f16 v158, v227, v36 op_sel_hi:[0,1]\n"
      "v_pk_mul_f16 v159, v227, v37 op_sel_hi:[0,1]\n"
      "v_pk_mul_f16 v160, v214, v22 op_sel_hi:[0,1]\n"
      "s_waitcnt lgkmcnt(1)\n"
      "v_mfma_f32_32x32x16_f16 v[48:63], v[152:155], v[164:167], v[48:63]\n"
      "ds_read_b128 v[148:151], v235 offset:3456\n"
      "v_pk_mul_f16 v161, v214, v23 op_sel_hi:[0,1]\n"
      "v_pk_mul_f16 v162, v214, v24 op_sel_hi:[0,1]\n"
      "v_pk_mul_f16 v163, v214, v25 op_sel_hi:[0,1]\n"
      "v_pk_mul_f16 v164, v227, v38 op_sel_hi:[0,1]\n"
      "v_mfma_f32_32x32x16_f16 v[64:79], v[152:155], v[156:159], v[64:79]\n"
      "v_pk_mul_f16 v165, v227, v39 op_sel_hi:[0,1]\n"
      "v_pk_mul_f16 v166, v227, v40 op_sel_hi:[0,1]\n"
      "v_pk_mul_f16 v167, v227, v41 op_sel_hi:[0,1]\n"
      "v_pk_mul_f16 v156, v214, v26 op_sel_hi:[0,1]\n"
      "s_waitcnt lgkmcnt(1)\n"
      "v_mfma_f32_32x32x16_f16 v[48:63], v[144:147], v[160:163], v[48:63]\n"
      "ds_read_b128 v[152:155], v235 offset:4320\n"
      "v_pk_mul_f16 v157, v214, v27 op_sel_hi:[0,1]\n"
      "v_pk_mul_f16 v158, v214, v28 op_sel_hi:[0,1]\n"
      "v_pk_mul_f16 v159, v215, v16 op_sel_hi:[0,1]\n"
      "v_pk_mul_f16 v160, v227, v42 op_sel_hi:[0,1]\n"
      "v_mfma_f32_32x32x16_f16 v[64:79], v[144:147], v[164:167], v[64:79]\n"
      "v_pk_mul_f16 v161, v227, v43 op_sel_hi:[0,1]\n"
      "v_pk_mul_f16 v162, v227, v44 op_sel_hi:[0,1]\n"
      "v_pk_mul_f16 v163, v228, v32 op_sel_hi:[0,1]\n"
      "v_pk_mul_f16 v164, v215, v17 op_sel_hi:[0,1]\n"
      "s_waitcnt lgkmcnt(1)\n"
      "v_mfma_f32_32x32x16_f16 v[48:63], v[148:151], v[156:159], v[48:63]\n"
      "ds_read_b128 v[144:147], v235 offset:5184\n"
      "v_pk_mul_f16 v165, v215, v18 op_sel_hi:[0,1]\n"
      "v_pk_mul_f16 v166, v215, v19 op_sel_hi:[0,1]\n"
      "v_pk_mul_f16 v167, v215, v20 op_sel_hi:[0,1]\n"
      "v_pk_mul_f16 v156, v228, v33 op_sel_hi:[0,1]\n"
      "v_mfma_f32_32x32x16_f16 v[64:79], v[148:151], v[160:163], v[64:79]\n"
      "v_pk_mul_f16 v157, v228, v34 op_sel_hi:[0,1]\n"
      "v_pk_mul_f16 v158, v228, v35 op_sel_hi:[0,1]\n"
      "v_pk_mul_f16 v159, v228, v36 op_sel_hi:[0,1]\n"
      "v_pk_mul_f16 v160, v215, v21 op_sel_hi:[0,1]\n"
      "s_waitcnt lgkmcnt(1)\n"
      "v_mfma_f32_32x32x16_f16 v[48:63], v[152:155], v[164:167], v[48:63]\n"
      "ds_read_b128 v[148:151], v235 offset:6048\n"
      "v_pk_mul_f16 v161, v215, v22 op_sel_hi:[0,1]\n"
      "v_pk_mul_f16 v162, v215, v23 op_sel_hi:[0,1]\n"
      "v_pk_mul_f16 v163, v215, v24 op_sel_hi:[0,1]\n"
      "v_pk_mul_f16 v164, v228, v37 op_sel_hi:[0,1]\n"
      "v_mfma_f32_32x32x16_f16 v[64:79], v[152:155], v[156:159], v[64:79]\n"
      "v_pk_mul_f16 v165, v228, v38 op_sel_hi:[0,1]\n"
      "v_pk_mul_f16 v166, v228, v39 op_sel_hi:[0,1]\n"
      "v_pk_mul_f16 v167, v228, v40 op_sel_hi:[0,1]\n"
      "v_pk_mul_f16 v156, v215, v25 op_sel_hi:[0,1]\n"
      "s_waitcnt lgkmcnt(1)\n"
      "v_mfma_f32_32x32x16_f16 v[48:63], v[144:147], v[160:163], v[48:63]\n"
      "ds_read_b128 v[152:155], v235 offset:6912\n"
      "v_pk_mul_f16 v157, v215, v26 op_sel_hi:[0,1]\n"
      "v_pk_mul_f16 v158, v215, v27 op_sel_hi:[0,1]\n"
      "v_pk_mul_f16 v159, v215, v28 op_sel_hi:[0,1]\n"
      "v_pk_mul_f16 v160, v228, v41 op_sel_hi:[0,1]\n"
      "v_mfma_f32_32x32x16_f16 v[64:79], v[144:147], v[164:167], v[64:79]\n"
      "v_pk_mul_f16 v161, v228, v42 op_sel_hi:[0,1]\n"
      "v_pk_mul_f16 v162, v228, v43 op_sel_hi:[0,1]\n"
      "v_pk_mul_f16 v163, v228, v44 op_sel_hi:[0,1]\n"
      "v_pk_mul_f16 v164, v216, v16 op_sel_hi:[0,1]\n"
      "s_waitcnt lgkmcnt(1)\n"
      "v_mfma_f32_32x32x16_f16 v[48:63], v[148:151], v[156:159], v[48:63]\n"
      "ds_read_b128 v[144:147], v235 offset:7776\n"
      "v_pk_mul_f16 v165, v216, v17 op_sel_hi:[0,1]\n"
      "v_pk_mul_f16 v166, v216, v18 op_sel_hi:[0,1]\n"
      "v_pk_mul_f16 v167, v216, v19 op_sel_hi:[0,1]\n"
      "v_pk_mul_f16 v156, v229, v32 op_sel_hi:[0,1]\n"
      "v_mfma_f32_32x32x16_f16 v[64:79], v[148:151], v[160:163], v[64:79]\n"
      "v_pk_mul_f16 v157, v229, v33 op_sel_hi:[0,1]\n"
      "v_pk_mul_f16 v158, v229, v34 op_sel_hi:[0,1]\n"
      "v_pk_mul_f16 v159, v229, v35 op_sel_hi:[0,1]\n"
      "v_pk_mul_f16 v160, v216, v20 op_sel_hi:[0,1]\n"
      "s_waitcnt lgkmcnt(1)\n"
      "v_mfma_f32_32x32x16_f16 v[48:63], v[152:155], v[164:167], v[48:63]\n"
      "ds_read_b128 v[148:151], v235 offset:8640\n"
      "v_pk_mul_f16 v161, v216, v21 op_sel_hi:[0,1]\n"
      "v_pk_mul_f16 v162, v216, v22 op_sel_hi:[0,1]\n"
      "v_pk_mul_f16 v163, v216, v23 op_sel_hi:[0,1]\n"
      "v_pk_mul_f16 v164, v229, v36 op_sel_hi:[0,1]\n"
      "v_mfma_f32_32x32x16_f16 v[64:79], v[152:155], v[156:159], v[64:79]\n"
      "v_pk_mul_f16 v165, v229, v37 op_sel_hi:[0,1]\n"
      "v_pk_mul_f16 v166, v229, v38 op_sel_hi:[0,1]\n"
      "v_pk_mul_f16 v167, v229, v39 op_sel_hi:[0,1]\n"
      "v_pk_mul_f16 v156, v216, v24 op_sel_hi:[0,1]\n"
      "s_waitcnt lgkmcnt(1)\n"
      "v_mfma_f32_32x32x16_f16 v[48:63], v[144:147], v[160:163], v[48:63]\n"
      "ds_read_b128 v[152:155], v235 offset:9504\n"
      "v_pk_mul_f16 v157, v216, v25 op_sel_hi:[0,1]\n"
      "v_pk_mul_f16 v158, v216, v26 op_sel_hi:[0,1]\n"
      "v_pk_mul_f16 v159, v216, v27 op_sel_hi:[0,1]\n"
      "v_pk_mul_f16 v160, v229, v40 op_sel_hi:[0,1]\n"
      "v_mfma_f32_32x32x16_f16 v[64:79], v[144:147], v[164:167], v[64:79]\n"
      "v_pk_mul_f16 v161, v229, v41 op_sel_hi:[0,1]\n"
      "v_pk_mul_f16 v162, v229, v42 op_sel_hi:[0,1]\n"
      "v_pk_mul_f16 v163, v229, v43 op_sel_hi:[0,1]\n"
      "v_pk_mul_f16 v164, v216, v28 op_sel_hi:[0,1]\n"
      "s_waitcnt lgkmcnt(1)\n"
      "v_mfma_f32_32x32x16_f16 v[48:63], v[148:151], v[156:159], v[48:63]\n"
      "ds_read_b128 v[144:147], v235 offset:10368\n"
      "v_pk_mul_f16 v165, v217, v16 op_sel_hi:[0,1]\n"
      "v_pk_mul_f16 v166, v217, v17 op_sel_hi:[0,1]\n"
      "v_pk_mul_f16 v167, v217, v18 op_sel_hi:[0,1]\n"
      "v_pk_mul_f16 v156, v229, v44 op_sel_hi:[0,1]\n"
      "v_mfma_f32_32x32x16_f16 v[64:79], v[148:151], v[160:163], v[64:79]\n"
      "v_pk_mul_f16 v157, v230, v32 op_sel_hi:[0,1]\n"
      "v_pk_mul_f16 v158, v230, v33 op_sel_hi:[0,1]\n"
      "v_pk_mul_f16 v159, v230, v34 op_sel_hi:[0,1]\n"
      "v_pk_mul_f16 v160, v217, v19 op_sel_hi:[0,1]\n"
      "s_waitcnt lgkmcnt(1)\n"
      "v_mfma_f32_32x32x16_f16 v[48:63], v[152:155], v[164:167], v[48:63]\n"
      "ds_read_b128 v[148:151], v235 offset:11232\n"
      "v_pk_mul_f16 v161, v217, v20 op_sel_hi:[0,1]\n"
      "v_pk_mul_f16 v162, v217, v21 op_sel_hi:[0,1]\n"
      "v_pk_mul_f16 v163, v217, v22 op_sel_hi:[0,1]\n"
      "v_pk_mul_f16 v164, v230, v35 op_sel_hi:[0,1]\n"
      "v_mfma_f32_32x32x16_f16 v[64:79], v[152:155], v[156:159], v[64:79]\n"
      "v_pk_mul_f16 v165, v230, v36 op_sel_hi:[0,1]\n"
      "v_pk_mul_f16 v166, v230, v37 op_sel_hi:[0,1]\n"
      "v_pk_mul_f16 v167, v230, v38 op_sel_hi:[0,1]\n"
      "v_pk_mul_f16 v156, v217, v23 op_sel_hi:[0,1]\n"
      "s_waitcnt lgkmcnt(1)\n"
      "v_mfma_f32_32x32x16_f16 v[48:63], v[144:147], v[160:163], v[48:63]\n"
      "ds_read_b128 v[152:155], v235 offset:12096\n"
      "v_pk_mul_f16 v157, v217, v24 op_sel_hi:[0,1]\n"
      "v_pk_mul_f16 v158, v217, v25 op_sel_hi:[0,1]\n"
      "v_pk_mul_f16 v159, v217, v26 op_sel_hi:[0,1]\n"
      "v_pk_mul_f16 v160, v230, v39 op_sel_hi:[0,1]\n"
      "v_mfma_f32_32x32x16_f16 v[64:79], v[144:147], v[164:167], v[64:79]\n"
      "v_pk_mul_f16 v161, v230, v40 op_sel_hi:[0,1]\n"
      "v_pk_mul_f16 v162, v230, v41 op_sel_hi:[0,1]\n"
      "v_pk_mul_f16 v163, v230, v42 op_sel_hi:[0,1]\n"
      "v_pk_mul_f16 v164, v217, v27 op_sel_hi:[0,1]\n"
      "s_waitcnt lgkmcnt(1)\n"
      "v_mfma_f32_32x32x16_f16 v[48:63], v[148:151], v[156:159], v[48:63]\n"
      "ds_read_b128 v[144:147], v235 offset:12960\n"
      "v_pk_mul_f16 v165, v217, v28 op_sel_hi:[0,1]\n"
      "v_pk_mul_f16 v166, v218, v16 op_sel_hi:[0,1]\n"
      "v_pk_mul_f16 v167, v218, v17 op_sel_hi:[0,1]\n"
      "v_pk_mul_f16 v156, v230, v43 op_sel_hi:[0,1]\n"
      "v_mfma_f32_32x32x16_f16 v[64:79], v[148:151], v[160:163], v[64:79]\n"
      "v_pk_mul_f16 v157, v230, v44 op_sel_hi:[0,1]\n"
      "v_pk_mul_f16 v158, v231, v32 op_sel_hi:[0,1]\n"
      "v_pk_mul_f16 v159, v231, v33 op_sel_hi:[0,1]\n"
      "v_pk_mul_f16 v160, v218, v18 op_sel_hi:[0,1]\n"
      "s_waitcnt lgkmcnt(1)\n"
      "v_mfma_f32_32x32x16_f16 v[48:63], v[152:155], v[164:167], v[48:63]\n"
      "ds_read_b128 v[148:151], v235 offset:13824\n"
      "v_pk_mul_f16 v161, v218, v19 op_sel_hi:[0,1]\n"
      "v_pk_mul_f16 v162, v218, v20 op_sel_hi:[0,1]\n"
      "v_pk_mul_f16 v163, v218, v21 op_sel_hi:[0,1]\n"
      "v_pk_mul_f16 v164, v231, v34 op_sel_hi:[0,1]\n"
      "v_mfma_f32_32x32x16_f16 v[64:79], v[152:155], v[156:159], v[64:79]\n"
      "v_pk_mul_f16 v165, v231, v35 op_sel_hi:[0,1]\n"
      "v_pk_mul_f16 v166, v231, v36 op_sel_hi:[0,1]\n"
      "v_pk_mul_f16 v167, v231, v37 op_sel_hi:[0,1]\n"
      "v_pk_mul_f16 v156, v218, v22 op_sel_hi:[0,1]\n"
      "s_waitcnt lgkmcnt(1)\n"
      "v_mfma_f32_32x32x16_f16 v[48:63], v[144:147], v[160:163], v[48:63]\n"
      "ds_read_b128 v[152:155], v235 offset:14688\n"
      "v_pk_mul_f16 v157, v218, v23 op_sel_hi:[0,1]\n"
      "v_pk_mul_f16 v158, v218, v24 op_sel_hi:[0,1]\n"
      "v_pk_mul_f16 v159, v218, v25 op_sel_hi:[0,1]\n"
      "v_pk_mul_f16 v160, v231, v38 op_sel_hi:[0,1]\n"
      "v_mfma_f32_32x32x16_f16 v[64:79], v[144:147], v[164:167], v[64:79]\n"
      "v_pk_mul_f16 v161, v231, v39 op_sel_hi:[0,1]\n"
      "v_pk_mul_f16 v162, v231, v40 op_sel_hi:[0,1]\n"
      "v_pk_mul_f16 v163, v231, v41 op_sel_hi:[0,1]\n"
      "v_pk_mul_f16 v164, v218, v26 op_sel_hi:[0,1]\n"
      "s_waitcnt lgkmcnt(1)\n"
      "v_mfma_f32_32x32x16_f16 v[48:63], v[148:151], v[156:159], v[48:63]\n"
      "ds_read_b128 v[144:147], v235 offset:15552\n"
      "v_pk_mul_f16 v165, v218, v27 op_sel_hi:[0,1]\n"
      "v_pk_mul_f16 v166, v218, v28 op_sel_hi:[0,1]\n"
      "v_pk_mul_f16 v167, v219, v16 op_sel_hi:[0,1]\n"
      "v_pk_mul_f16 v156, v231, v42 op_sel_hi:[0,1]\n"
      "v_mfma_f32_32x32x16_f16 v[64:79], v[148:151], v[160:163], v[64:79]\n"
      "v_pk_mul_f16 v157, v231, v43 op_sel_hi:[0,1]\n"
      "v_pk_mul_f16 v158, v231, v44 op_sel_hi:[0,1]\n"
      "v_pk_mul_f16 v159, v232, v32 op_sel_hi:[0,1]\n"
      "v_pk_mul_f16 v160, v219, v17 op_sel_hi:[0,1]\n"
      "s_waitcnt lgkmcnt(1)\n"
      "v_mfma_f32_32x32x16_f16 v[48:63], v[152:155], v[164:167], v[48:63]\n"
      "ds_read_b128 v[148:151], v235 offset:16416\n"
      "v_pk_mul_f16 v161, v219, v18 op_sel_hi:[0,1]\n"
      "v_pk_mul_f16 v162, v219, v19 op_sel_hi:[0,1]\n"
      "v_pk_mul_f16 v163, v219, v20 op_sel_hi:[0,1]\n"
      "v_pk_mul_f16 v164, v232, v33 op_sel_hi:[0,1]\n"
      "v_mfma_f32_32x32x16_f16 v[64:79], v[152:155], v[156:159], v[64:79]\n"
      "v_pk_mul_f16 v165, v232, v34 op_sel_hi:[0,1]\n"
      "v_pk_mul_f16 v166, v232, v35 op_sel_hi:[0,1]\n"
      "v_pk_mul_f16 v167, v232, v36 op_sel_hi:[0,1]\n"
      "v_pk_mul_f16 v156, v219, v21 op_sel_hi:[0,1]\n"
      "s_waitcnt lgkmcnt(1)\n"
      "v_mfma_f32_32x32x16_f16 v[48:63], v[144:147], v[160:163], v[48:63]\n"
      "ds_read_b128 v[152:155], v235 offset:17280\n"
      "v_pk_mul_f16 v157, v219, v22 op_sel_hi:[0,1]\n"
      "v_pk_mul_f16 v158, v219, v23 op_sel_hi:[0,1]\n"
      "v_pk_mul_f16 v159, v219, v24 op_sel_hi:[0,1]\n"
      "v_pk_mul_f16 v160, v232, v37 op_sel_hi:[0,1]\n"
      "v_mfma_f32_32x32x16_f16 v[64:79], v[144:147], v[164:167], v[64:79]\n"
      "v_pk_mul_f16 v161, v232, v38 op_sel_hi:[0,1]\n"
      "v_pk_mul_f16 v162, v232, v39 op_sel_hi:[0,1]\n"
      "v_pk_mul_f16 v163, v232, v40 op_sel_hi:[0,1]\n"
      "v_pk_mul_f16 v164, v219, v25 op_sel_hi:[0,1]\n"
      "s_waitcnt lgkmcnt(1)\n"
      "v_mfma_f32_32x32x16_f16 v[48:63], v[148:151], v[156:159], v[48:63]\n"
      "ds_read_b128 v[144:147], v235 offset:18144\n"
      "v_pk_mul_f16 v165, v219, v26 op_sel_hi:[0,1]\n"
      "v_pk_mul_f16 v166, v219, v27 op_sel_hi:[0,1]\n"
      "v_pk_mul_f16 v167, v219, v28 op_sel_hi:[0,1]\n"
      "v_pk_mul_f16 v156, v232, v41 op_sel_hi:[0,1]\n"
      "v_mfma_f32_32x32x16_f16 v[64:79], v[148:151], v[160:163], v[64:79]\n"
      "v_pk_mul_f16 v157, v232, v42 op_sel_hi:[0,1]\n"
      "v_pk_mul_f16 v158, v232, v43 op_sel_hi:[0,1]\n"
      "v_pk_mul_f16 v159, v232, v44 op_sel_hi:[0,1]\n"
      "v_pk_mul_f16 v160, v220, v16 op_sel_hi:[0,1]\n"
      "s_waitcnt lgkmcnt(1)\n"
      "v_mfma_f32_32x32x16_f16 v[48:63], v[152:155], v[164:167], v[48:63]\n"
      "ds_read_b128 v[148:151], v235 offset:19008\n"
      "v_pk_mul_f16 v161, v220, v17 op_sel_hi:[0,1]\n"
      "v_pk_mul_f16 v162, v220, v18 op_sel_hi:[0,1]\n"
      "v_pk_mul_f16 v163, v220, v19 op_sel_hi:[0,1]\n"
      "v_pk_mul_f16 v164, v233, v32 op_sel_hi:[0,1]\n"
      "v_mfma_f32_32x32x16_f16 v[64:79], v[152:155], v[156:159], v[64:79]\n"
      "v_pk_mul_f16 v165, v233, v33 op_sel_hi:[0,1]\n"
      "v_pk_mul_f16 v166, v233, v34 op_sel_hi:[0,1]\n"
      "v_pk_mul_f16 v167, v233, v35 op_sel_hi:[0,1]\n"
      "v_pk_mul_f16 v156, v220, v20 op_sel_hi:[0,1]\n"
      "s_waitcnt lgkmcnt(1)\n"
      "v_mfma_f32_32x32x16_f16 v[48:63], v[144:147], v[160:163], v[48:63]\n"
      "ds_read_b128 v[152:155], v235 offset:19872\n"
      "v_pk_mul_f16 v157, v220, v21 op_sel_hi:[0,1]\n"
      "v_pk_mul_f16 v158, v220, v22 op_sel_hi:[0,1]\n"
      "v_pk_mul_f16 v159, v220, v23 op_sel_hi:[0,1]\n"
      "v_pk_mul_f16 v160, v233, v36 op_sel_hi:[0,1]\n"
      "v_mfma_f32_32x32x16_f16 v[64:79], v[144:147], v[164:167], v[64:79]\n"
      "v_pk_mul_f16 v161, v233, v37 op_sel_hi:[0,1]\n"
      "v_pk_mul_f16 v162, v233, v38 op_sel_hi:[0,1]\n"
      "v_pk_mul_f16 v163, v233, v39 op_sel_hi:[0,1]\n"
      "v_pk_mul_f16 v164, v220, v24 op_sel_hi:[0,1]\n"
      "s_waitcnt lgkmcnt(1)\n"
      "v_mfma_f32_32x32x16_f16 v[48:63], v[148:151], v[156:159], v[48:63]\n"
      "ds_read_b128 v[144:147], v235 offset:20736\n"
      "v_pk_mul_f16 v165, v220, v25 op_sel_hi:[0,1]\n"
      "v_pk_mul_f16 v166, v220, v26 op_sel_hi:[0,1]\n"
      "v_pk_mul_f16 v167, v220, v27 op_sel_hi:[0,1]\n"
      "v_pk_mul_f16 v156, v233, v40 op_sel_hi:[0,1]\n"
      "v_mfma_f32_32x32x16_f16 v[64:79], v[148:151], v[160:163], v[64:79]\n"
      "v_pk_mul_f16 v157, v233, v41 op_sel_hi:[0,1]\n"
      "v_pk_mul_f16 v158, v233, v42 op_sel_hi:[0,1]\n"
      "v_pk_mul_f16 v159, v233, v43 op_sel_hi:[0,1]\n"
      "v_pk_mul_f16 v160, v220, v28 op_sel_hi:[0,1]\n"
      "s_waitcnt lgkmcnt(1)\n"
      "v_mfma_f32_32x32x16_f16 v[48:63], v[152:155], v[164:167], v[48:63]\n"
      "v_pk_mul_f16 v164, v233, v44 op_sel_hi:[0,1]\n"
      "v_mfma_f32_32x32x16_f16 v[64:79], v[152:155], v[156:159], v[64:79]\n"
      "s_waitcnt lgkmcnt(0)\n"
      "v_mfma_f32_32x32x16_f16 v[48:63], v[144:147], v[160:163], v[48:63]\n"
      "v_mfma_f32_32x32x16_f16 v[64:79], v[144:147], v[164:167], v[64:79]\n"
      "s_nop 3\n"
      "v_mul_f32 v242, v48, v80\n"
      "v_mul_f32 v243, v49, v81\n"
      "v_fmac_f32 v242, v50, v82\n"
      "v_fmac_f32 v243, v51, v83\n"
      "v_fmac_f32 v242, v52, v84\n"
      "v_fmac_f32 v243, v53, v85\n"
      "v_fmac_f32 v242, v54, v86\n"
      "s_nop 0\n"
      "v_mul_f32 v246, v64, v96\n"
      "v_fmac_f32 v243, v55, v87\n"
      "v_mul_f32 v247, v65, v97\n"
      "v_fmac_f32 v242, v56, v88\n"
      "v_fmac_f32 v246, v66, v98\n"
      "v_fmac_f32 v243, v57, v89\n"
      "v_fmac_f32 v247, v67, v99\n"
      "v_fmac_f32 v242, v58, v90\n"
      "v_fmac_f32 v246, v68, v100\n"
      "v_fmac_f32 v243, v59, v91\n"
      "v_fmac_f32 v247, v69, v101\n"
      "v_fmac_f32 v242, v60, v92\n"
      "v_fmac_f32 v246, v70, v102\n"
      "v_ldexp_f32 v245, v61, v245\n"
      "v_fmac_f32 v247, v71, v103\n"
      "v_add_f32 v242, v242, v243\n"
      "v_fmac_f32 v246, v72, v104\n"
      "v_add_f32 v240, v240, v245\n"
      "v_fmac_f32 v247, v73, v105\n"
      "v_ldexp_f32 v242, v242, v238\n"
      "v_fmac_f32 v246, v74, v106\n"
      "v_add_f32 v240, v240, v242\n"
      "v_fmac_f32 v247, v75, v107\n"
      "v_fmac_f32 v246, v76, v108\n"
      "v_ldexp_f32 v249, v77, v249\n"
      "v_add_f32 v246, v246, v247\n"
      "v_add_f32 v241, v241, v249\n"
      "v_ldexp_f32 v246, v246, v239\n"
      "v_add_f32 v241, v241, v246\n"
      "s_nop 1\n"
      "v_permlane32_swap_b32 v240, v241\n"
      "v_add_f32 v240, v240, v241\n"
      "s_nop 1\n"
      "v_add_f32_dpp v240, v240, v240 row_ror:8 row_mask:0xf bank_mask:0xf\n"
      "s_nop 1\n"
      "v_add_f32_dpp v240, v240, v240 row_ror:4 row_mask:0xf bank_mask:0xf\n"
      "s_nop 1\n"
      "v_add_f32_dpp v240, v240, v240 row_ror:2 row_mask:0xf bank_mask:0xf\n"
      "s_nop 1\n"
      "v_add_f32_dpp v240, v240, v240 row_ror:1 row_mask:0xf bank_mask:0xf\n"
      "v_add_f32 v240, v240, v204\n"
      "s_mov_b64 exec, s[72:73]\n"
      "v_cvt_pk_bf16_f32 v240, v240, v240\n"
      "global_store_short v251, v240, s[74:75]\n"
      "s_mov_b64 exec, -1\n"
      "s_branch x_done\n"
      "q_slow1:\n"
      "s_mov_b32 s88, 0x400000\n"
      "9:\n"
      "ds_read_b32 v206, v251 offset:140\n"
      "s_sub_u32 s88, s88, 1\n"
      "s_waitcnt lgkmcnt(0)\n"
      "s_cbranch_scc1 q_back1\n"
      "v_cmp_gt_u32 vcc, s86, v206\n"
      "s_cbranch_vccnz 9b\n"
      "s_branch q_back1\n"
      "q_slow:\n"
      "s_mov_b32 s88, 0x400000\n"
      "9:\n"
      "s_sleep 1\n"
      "ds_read_b32 v205, v251 offset:136\n"
      "s_sub_u32 s88, s88, 1\n"
      "s_waitcnt lgkmcnt(0)\n"
      "s_cbranch_scc1 q_back\n"
      "v_cmp_gt_u32 vcc, 4, v205\n"
      "s_cbranch_vccnz 9b\n"
      "s_branch q_back\n"
      "xl_body:\n"
      "s_waitcnt vmcnt(0)\n"
      "s_barrier\n"
      "ds_read_b128 v[112:115], v12\n"
      "ds_read_b64_tr_b16 v[216:217], v4 offset:0\n"
      "ds_read_b64_tr_b16 v[218:219], v4 offset:256\n"
      "ds_read_b128 v[116:119], v13\n"
      "ds_read_b64_tr_b16 v[220:221], v4 offset:1024\n"
      "ds_read_b64_tr_b16 v[222:223], v4 offset:1280\n"
      "ds_read_b128 v[120:123], v14\n"
      "ds_read_b64_tr_b16 v[224:225], v4 offset:2048\n"
      "ds_read_b64_tr_b16 v[226:227], v4 offset:2304\n"
      "s_waitcnt lgkmcnt(14)\n"
      "ds_read_b128 v[124:127], v15\n"
      "s_waitcnt lgkmcnt(14)\n"
      "ds_read_b64_tr_b16 v[228:229], v4 offset:3072\n"
      "s_waitcnt lgkmcnt(14)\n"
      "ds_read_b64_tr_b16 v[230:231], v4 offset:3328\n"
      "s_waitcnt lgkmcnt(14)\n"
      "ds_read_b128 v[128:131], v16\n"
      "s_waitcnt lgkmcnt(14)\n"
      "ds_read_b64_tr_b16 v[232:233], v4 offset:4096\n"
      "s_waitcnt lgkmcnt(14)\n"
      "ds_read_b64_tr_b16 v[234:235], v4 offset:4352\n"
      "s_waitcnt lgkmcnt(12)\n"
      "v_mfma_f32_32x32x16_bf16 v[48:63], v[216:219], v[112:115], v[48:63]\n"
      "s_add_u32 m0, s62, 26624\n"
      "v_add_u32 v46, 0, v9\n"
      "global_load_lds_dwordx4 v46, s[64:65]\n"
      "ds_read_b128 v[132:135], v17\n"
      "ds_read_b64_tr_b16 v[236:237], v4 offset:5120\n"
      "ds_read_b64_tr_b16 v[238:239], v4 offset:5376\n"
      "s_waitcnt lgkmcnt(12)\n"
      "v_mfma_f32_32x32x16_bf16 v[48:63], v[220:223], v[116:119], v[48:63]\n"
      "s_add_u32 m0, s62, 30720\n"
      "v_add_u32 v46, 53248, v9\n"
      "global_load_lds_dwordx4 v46, s[64:65]\n"
      "ds_read_b128 v[136:139], v18\n"
      "ds_read_b64_tr_b16 v[216:217], v4 offset:6144\n"
      "ds_read_b64_tr_b16 v[218:219], v4 offset:6400\n"
      "s_waitcnt lgkmcnt(12)\n"
      "v_mfma_f32_32x32x16_bf16 v[48:63], v[224:227], v[120:123], v[48:63]\n"
      "s_add_u32 m0, s62, 34816\n"
      "v_add_u32 v46, 106496, v9\n"
      "global_load_lds_dwordx4 v46, s[64:65]\n"
      "ds_read_b128 v[140:143], v19\n"
      "ds_read_b64_tr_b16 v[220:221], v4 offset:7168\n"
      "ds_read_b64_tr_b16 v[222:223], v4 offset:7424\n"
      "s_waitcnt lgkmcnt(12)\n"
      "v_mfma_f32_32x32x16_bf16 v[48:63], v[228:231], v[124:127], v[48:63]\n"
      "s_add_u32 m0, s62, 38912\n"
      "v_add_u32 v46, 159744, v9\n"
      "global_load_lds_dwordx4 v46, s[64:65]\n"
      "s_barrier\n"
      "ds_read_b128 v[144:147], v20\n"
      "ds_read_b64_tr_b16 v[224:225], v4 offset:8192\n"
      "ds_read_b64_tr_b16 v[226:227], v4 offset:8448\n"
      "s_waitcnt lgkmcnt(12)\n"
      "v_mfma_f32_32x32x16_bf16 v[48:63], v[232:235], v[128:131], v[48:63]\n"
      "s_add_u32 m0, s62, 43008\n"
      "v_add_u32 v46, 212992, v9\n"
      "global_load_lds_dwordx4 v46, s[64:65]\n"
      "ds_read_b128 v[148:151], v21\n"
      "ds_read_b64_tr_b16 v[228:229], v4 offset:9216\n"
      "ds_read_b64_tr_b16 v[230:231], v4 offset:9472\n"
      "s_waitcnt lgkmcnt(12)\n"
      "v_mfma_f32_32x32x16_bf16 v[48:63], v[236:239], v[132:135], v[48:63]\n"
      "s_add_u32 m0, s62, 47104\n"
      "v_add_u32 v46, 266240, v9\n"
      "global_load_lds_dwordx4 v46, s[64:65]\n"
      "ds_read_b128 v[152:155], v22\n"
      "ds_read_b64_tr_b16 v[232:233], v4 offset:10240\n"
      "ds_read_b64_tr_b16 v[234:235], v4 offset:10496\n"
      "s_waitcnt lgkmcnt(12)\n"
      "v_mfma_f32_32x32x16_bf16 v[48:63], v[216:219], v[136:139], v[48:63]\n"
      "s_mul_i32 s88, s63, 13312\n"
      "s_lshl_b32 m0, s63, 10\n"
      "v_add_u32 v46, s88, v9\n"
      "s_add_u32 m0, m0, s62\n"
      "s_add_u32 m0, m0, 26624\n"
      "s_nop 0\n"
      "global_load_lds_dwordx4 v46, s[64:65]\n"
      "ds_read_b128 v[156:159], v23\n"
      "ds_read_b64_tr_b16 v[236:237], v4 offset:11264\n"
      "ds_read_b64_tr_b16 v[238:239], v4 offset:11520\n"
      "s_waitcnt lgkmcnt(12)\n"
      "v_mfma_f32_32x32x16_bf16 v[48:63], v[220:223], v[140:143], v[48:63]\n"
      "ds_read_b128 v[160:163], v24\n"
      "ds_read_b64_tr_b16 v[216:217], v4 offset:12288\n"
      "ds_read_b64_tr_b16 v[218:219], v4 offset:12544\n"
      "v_readfirstlane_b32 s90, v44\n"
      "v_readfirstlane_b32 s91, v45\n"
      "s_nop 0\n"
      "s_waitcnt lgkmcnt(12)\n"
      "v_mfma_f32_32x32x16_bf16 v[48:63], v[224:227], v[144:147], v[48:63]\n"
      "ds_read_b128 v[164:167], v25\n"
      "ds_read_b64_tr_b16 v[220:221], v4 offset:13312\n"
      "ds_read_b64_tr_b16 v[222:223], v4 offset:13568\n"
      "s_add_u32 s90, s90, 0x9e3779b1\n"
      "s_add_u32 s91, s91, 0x85ebca77\n"
      "v_lshlrev_b32 v64, 16, v72\n"
      "s_waitcnt lgkmcnt(12)\n"
      "v_mfma_f32_32x32x16_bf16 v[48:63], v[228:231], v[148:151], v[48:63]\n"
      "ds_read_b128 v[168:171], v26\n"
      "ds_read_b64_tr_b16 v[224:225], v4 offset:14336\n"
      "ds_read_b64_tr_b16 v[226:227], v4 offset:14592\n"
      "v_and_b32 v65, 0xffff0000, v72\n"
      "v_lshlrev_b32 v66, 16, v73\n"
      "v_and_b32 v67, 0xffff0000, v73\n"
      "s_waitcnt lgkmcnt(12)\n"
      "v_mfma_f32_32x32x16_bf16 v[48:63], v[232:235], v[152:155], v[48:63]\n"
      "ds_read_b128 v[172:175], v27\n"
      "ds_read_b64_tr_b16 v[228:229], v4 offset:15360\n"
      "ds_read_b64_tr_b16 v[230:231], v4 offset:15616\n"
      "v_lshlrev_b32 v68, 16, v74\n"
      "v_and_b32 v69, 0xffff0000, v74\n"
      "v_lshlrev_b32 v70, 16, v75\n"
      "s_waitcnt lgkmcnt(12)\n"
      "v_mfma_f32_32x32x16_bf16 v[48:63], v[236:239], v[156:159], v[48:63]\n"
      "ds_read_b128 v[176:179], v28\n"
      "ds_read_b64_tr_b16 v[232:233], v4 offset:16384\n"
      "ds_read_b64_tr_b16 v[234:235], v4 offset:16640\n"
      "v_and_b32 v71, 0xffff0000, v75\n"
      "v_lshlrev_b32 v72, 16, v76\n"
      "v_and_b32 v73, 0xffff0000, v76\n"
      "s_waitcnt lgkmcnt(12)\n"
      "v_mfma_f32_32x32x16_bf16 v[48:63], v[216:219], v[160:163], v[48:63]\n"
      "ds_read_b128 v[180:183], v29\n"
      "ds_read_b64_tr_b16 v[236:237], v4 offset:17408\n"
      "ds_read_b64_tr_b16 v[238:239], v4 offset:17664\n"
      "v_lshlrev_b32 v74, 16, v77\n"
      "v_and_b32 v75, 0xffff0000, v77\n"
      "v_lshlrev_b32 v76, 16, v78\n"
      "s_waitcnt lgkmcnt(12)\n"
      "v_mfma_f32_32x32x16_bf16 v[48:63], v[220:223], v[164:167], v[48:63]\n"
      "ds_read_b128 v[184:187], v30\n"
      "ds_read_b64_tr_b16 v[216:217], v4 offset:18432\n"
      "ds_read_b64_tr_b16 v[218:219], v4 offset:18688\n"
      "v_and_b32 v77, 0xffff0000, v78\n"
      "v_lshlrev_b32 v78, 16, v79\n"
      "v_and_b32 v79, 0xffff0000, v79\n"
      "s_waitcnt lgkmcnt(12)\n"
      "v_mfma_f32_32x32x16_bf16 v[48:63], v[224:227], v[168:171], v[48:63]\n"
      "ds_read_b128 v[188:191], v31\n"
      "ds_read_b64_tr_b16 v[220:221], v4 offset:19456\n"
      "ds_read_b64_tr_b16 v[222:223], v4 offset:19712\n"
      "v_lshlrev_b32 v80, 16, v88\n"
      "v_and_b32 v81, 0xffff0000, v88\n"
      "v_lshlrev_b32 v82, 16, v89\n"
      "s_waitcnt lgkmcnt(12)\n"
      "v_mfma_f32_32x32x16_bf16 v[48:63], v[228:231], v[172:175], v[48:63]\n"
      "ds_read_b128 v[192:195], v32\n"
      "ds_read_b64_tr_b16 v[224:225], v4 offset:20480\n"
      "ds_read_b64_tr_b16 v[226:227], v4 offset:20736\n"
      "v_and_b32 v83, 0xffff0000, v89\n"
      "v_lshlrev_b32 v84, 16, v90\n"
      "v_and_b32 v85, 0xffff0000, v90\n"
      "s_waitcnt lgkmcnt(12)\n"
      "v_mfma_f32_32x32x16_bf16 v[48:63], v[232:235], v[176:179], v[48:63]\n"
      "ds_read_b128 v[196:199], v33\n"
      "ds_read_b64_tr_b16 v[228:229], v4 offset:21504\n"
      "ds_read_b64_tr_b16 v[230:231], v4 offset:21760\n"
      "v_lshlrev_b32 v86, 16, v91\n"
      "v_and_b32 v87, 0xffff0000, v91\n"
      "v_lshlrev_b32 v88, 16, v92\n"
      "s_waitcnt lgkmcnt(12)\n"
      "v_mfma_f32_32x32x16_bf16 v[48:63], v[236:239], v[180:183], v[48:63]\n"
      "s_waitcnt vmcnt(0)\n"
      "s_mov_b64 exec, 1\n"
      "ds_add_u32 v40, v41 offset:140\n"
      "s_mov_b64 exec, -1\n"
      "ds_read_b128 v[200:203], v34\n"
      "ds_read_b64_tr_b16 v[232:233], v4 offset:22528\n"
      "s_waitcnt lgkmcnt(14)\n"
      "ds_read_b64_tr_b16 v[234:235], v4 offset:22784\n"
      "v_and_b32 v89, 0xffff0000, v92\n"
      "v_lshlrev_b32 v90, 16, v93\n"
      "v_and_b32 v91, 0xffff0000, v93\n"
      "s_waitcnt lgkmcnt(13)\n"
      "v_mfma_f32_32x32x16_bf16 v[48:63], v[216:219], v[184:187], v[48:63]\n"
      "ds_read_b128 v[204:207], v35\n"
      "ds_read_b64_tr_b16 v[236:237], v4 offset:23552\n"
      "s_waitcnt lgkmcnt(14)\n"
      "ds_read_b64_tr_b16 v[238:239], v4 offset:23808\n"
      "v_lshlrev_b32 v92, 16, v94\n"
      "v_and_b32 v93, 0xffff0000, v94\n"
      "v_lshlrev_b32 v94, 16, v95\n"
      "s_waitcnt lgkmcnt(13)\n"
      "v_mfma_f32_32x32x16_bf16 v[48:63], v[220:223], v[188:191], v[48:63]\n"
      "ds_read_b32 v42, v40 offset:140\n"
      "ds_read_b128 v[208:211], v36\n"
      "s_waitcnt lgkmcnt(14)\n"
      "ds_read_b64_tr_b16 v[216:217], v4 offset:24576\n"
      "s_waitcnt lgkmcnt(14)\n"
      "ds_read_b64_tr_b16 v[218:219], v4 offset:24832\n"
      "v_and_b32 v95, 0xffff0000, v95\n"
      "v_lshlrev_b32 v96, 16, v104\n"
      "v_and_b32 v97, 0xffff0000, v104\n"
      "s_waitcnt lgkmcnt(14)\n"
      "v_mfma_f32_32x32x16_bf16 v[48:63], v[224:227], v[192:195], v[48:63]\n"
      "ds_read_b128 v[212:215], v37\n"
      "s_waitcnt lgkmcnt(14)\n"
      "ds_read_b64_tr_b16 v[220:221], v4 offset:25600\n"
      "s_waitcnt lgkmcnt(14)\n"
      "ds_read_b64_tr_b16 v[222:223], v4 offset:25856\n"
      "v_lshlrev_b32 v98, 16, v105\n"
      "v_and_b32 v99, 0xffff0000, v105\n"
      "v_lshlrev_b32 v100, 16, v106\n"
      "s_waitcnt lgkmcnt(14)\n"
      "v_mfma_f32_32x32x16_bf16 v[48:63], v[228:231], v[196:199], v[48:63]\n"
      "s_waitcnt lgkmcnt(6)\n"
      "v_cmp_gt_u32 vcc, 4, v42\n"
      "s_cbranch_vccnz p1_slow\n"
      "p1_back:\n"
      "s_setprio 3\n"
      "ds_read_b64_tr_b16 v[224:225], v4 offset:26624\n"
      "ds_read_b64_tr_b16 v[226:227], v4 offset:26880\n"
      "v_and_b32 v101, 0xffff0000, v106\n"
      "v_lshlrev_b32 v102, 16, v107\n"
      "v_and_b32 v103, 0xffff0000, v107\n"
      "v_mfma_f32_32x32x16_bf16 v[48:63], v[232:235], v[200:203], v[48:63]\n"
      "ds_read_b64_tr_b16 v[228:229], v4 offset:27648\n"
      "ds_read_b64_tr_b16 v[230:231], v4 offset:27904\n"
      "v_lshlrev_b32 v104, 16, v108\n"
      "v_and_b32 v105, 0xffff0000, v108\n"
      "v_lshlrev_b32 v106, 16, v109\n"
      "v_mfma_f32_32x32x16_bf16 v[48:63], v[236:239], v[204:207], v[48:63]\n"
      "ds_read_b64_tr_b16 v[232:233], v4 offset:28672\n"
      "ds_read_b64_tr_b16 v[234:235], v4 offset:28928\n"
      "v_and_b32 v107, 0xffff0000, v109\n"
      "v_lshlrev_b32 v108, 16, v110\n"
      "v_and_b32 v109, 0xffff0000, v110\n"
      "s_waitcnt lgkmcnt(9)\n"
      "v_mfma_f32_32x32x16_bf16 v[48:63], v[216:219], v[208:211], v[48:63]\n"
      "ds_read_b64_tr_b16 v[236:237], v4 offset:29696\n"
      "ds_read_b64_tr_b16 v[238:239], v4 offset:29952\n"
      "v_lshlrev_b32 v110, 16, v111\n"
      "v_and_b32 v111, 0xffff0000, v111\n"
      "s_waitcnt lgkmcnt(8)\n"
      "v_mfma_f32_32x32x16_bf16 v[48:63], v[220:223], v[212:215], v[48:63]\n"
      "ds_read_b64_tr_b16 v[216:217], v4 offset:30720\n"
      "ds_read_b64_tr_b16 v[218:219], v4 offset:30976\n"
      "s_waitcnt lgkmcnt(8)\n"
      "v_mfma_f32_32x32x16_bf16 v[64:79], v[224:227], v[112:115], v[64:79]\n"
      "ds_read_b64_tr_b16 v[220:221], v4 offset:31744\n"
      "ds_read_b64_tr_b16 v[222:223], v4 offset:32000\n"
      "s_waitcnt lgkmcnt(8)\n"
      "v_mfma_f32_32x32x16_bf16 v[64:79], v[228:231], v[116:119], v[64:79]\n"
      "ds_read_b64_tr_b16 v[224:225], v4 offset:32768\n"
      "ds_read_b64_tr_b16 v[226:227], v4 offset:33024\n"
      "v_mul_f32 v12, v48, v48\n"
      "v_mul_f32 v13, v49, v49\n"
      "v_fma_f32 v12, v12, v240, v241\n"
      "v_fma_f32 v13, v13, v240, v241\n"
      "v_mul_f32 v12, v12, v48\n"
      "s_waitcnt lgkmcnt(8)\n"
      "v_mfma_f32_32x32x16_bf16 v[64:79], v[232:235], v[120:123], v[64:79]\n"
      "ds_read_b64_tr_b16 v[228:229], v4 offset:33792\n"
      "ds_read_b64_tr_b16 v[230:231], v4 offset:34048\n"
      "v_mul_f32 v13, v13, v49\n"
      "v_exp_f32 v12, v12\n"
      "v_exp_f32 v13, v13\n"
      "s_waitcnt lgkmcnt(8)\n"
      "v_mfma_f32_32x32x16_bf16 v[64:79], v[236:239], v[124:127], v[64:79]\n"
      "ds_read_b64_tr_b16 v[232:233], v4 offset:34816\n"
      "ds_read_b64_tr_b16 v[234:235], v4 offset:35072\n"
      "v_mul_f32 v14, v48, v80\n"
      "v_mul_f32 v15, v49, v81\n"
      "v_add_f32 v12, 1.0, v12\n"
      "v_add_f32 v13, 1.0, v13\n"
      "v_rcp_f32 v12, v12\n"
      "s_waitcnt lgkmcnt(8)\n"
      "v_mfma_f32_32x32x16_bf16 v[64:79], v[216:219], v[128:131], v[64:79]\n"
      "ds_read_b64_tr_b16 v[236:237], v4 offset:35840\n"
      "ds_read_b64_tr_b16 v[238:239], v4 offset:36096\n"
      "v_rcp_f32 v13, v13\n"
      "v_mul_f32 v28, v12, v14\n"
      "v_mul_f32 v29, v13, v15\n"
      "v_mul_f32 v16, v50, v50\n"
      "s_waitcnt lgkmcnt(8)\n"
      "v_mfma_f32_32x32x16_bf16 v[64:79], v[220:223], v[132:135], v[64:79]\n"
      "ds_read_b64_tr_b16 v[216:217], v4 offset:36864\n"
      "ds_read_b64_tr_b16 v[218:219], v4 offset:37120\n"
      "v_mul_f32 v17, v51, v51\n"
      "v_fma_f32 v16, v16, v240, v241\n"
      "v_fma_f32 v17, v17, v240, v241\n"
      "v_mul_f32 v16, v16, v50\n"
      "v_mul_f32 v17, v17, v51\n"
      "s_waitcnt lgkmcnt(8)\n"
      "v_mfma_f32_32x32x16_bf16 v[64:79], v[224:227], v[136:139], v[64:79]\n"
      "ds_read_b64_tr_b16 v[220:221], v4 offset:37888\n"
      "ds_read_b64_tr_b16 v[222:223], v4 offset:38144\n"
      "v_exp_f32 v16, v16\n"
      "v_exp_f32 v17, v17\n"
      "v_mul_f32 v18, v50, v82\n"
      "s_waitcnt lgkmcnt(8)\n"
      "v_mfma_f32_32x32x16_bf16 v[64:79], v[228:231], v[140:143], v[64:79]\n"
      "ds_read_b64_tr_b16 v[224:225], v4 offset:38912\n"
      "ds_read_b64_tr_b16 v[226:227], v4 offset:39168\n"
      "v_mul_f32 v19, v51, v83\n"
      "v_add_f32 v16, 1.0, v16\n"
      "v_add_f32 v17, 1.0, v17\n"
      "v_rcp_f32 v16, v16\n"
      "s_waitcnt lgkmcnt(8)\n"
      "v_mfma_f32_32x32x16_bf16 v[64:79], v[232:235], v[144:147], v[64:79]\n"
      "ds_read_b64_tr_b16 v[228:229], v4 offset:39936\n"
      "ds_read_b64_tr_b16 v[230:231], v4 offset:40192\n"
      "v_rcp_f32 v17, v17\n"
      "v_fmac_f32 v28, v16, v18\n"
      "v_fmac_f32 v29, v17, v19\n"
      "v_mul_f32 v20, v52, v52\n"
      "s_waitcnt lgkmcnt(8)\n"
      "v_mfma_f32_32x32x16_bf16 v[64:79], v[236:239], v[148:151], v[64:79]\n"
      "ds_read_b64_tr_b16 v[232:233], v4 offset:40960\n"
      "ds_read_b64_tr_b16 v[234:235], v4 offset:41216\n"
      "v_mul_f32 v21, v53, v53\n"
      "v_fma_f32 v20, v20, v240, v241\n"
      "v_fma_f32 v21, v21, v240, v241\n"
      "v_mul_f32 v20, v20, v52\n"
      "v_mul_f32 v21, v21, v53\n"
      "s_waitcnt lgkmcnt(8)\n"
      "v_mfma_f32_32x32x16_bf16 v[64:79], v[216:219], v[152:155], v[64:79]\n"
      "ds_read_b64_tr_b16 v[236:237], v4 offset:41984\n"
      "ds_read_b64_tr_b16 v[238:239], v4 offset:42240\n"
      "global_load_dwordx4 v[112:115], v5, s[94:95] offset:1024 sc1\n"
      "v_exp_f32 v20, v20\n"
      "v_exp_f32 v21, v21\n"
      "v_mul_f32 v22, v52, v84\n"
      "s_waitcnt lgkmcnt(8)\n"
      "v_mfma_f32_32x32x16_bf16 v[64:79], v[220:223], v[156:159], v[64:79]\n"
      "ds_read_b64_tr_b16 v[216:217], v4 offset:43008\n"
      "ds_read_b64_tr_b16 v[218:219], v4 offset:43264\n"
      "global_load_dwordx4 v[116:119], v5, s[94:95] offset:1152 sc1\n"
      "v_mul_f32 v23, v53, v85\n"
      "v_add_f32 v20, 1.0, v20\n"
      "v_add_f32 v21, 1.0, v21\n"
      "v_rcp_f32 v20, v20\n"
      "s_waitcnt lgkmcnt(8)\n"
      "v_mfma_f32_32x32x16_bf16 v[64:79], v[224:227], v[160:163], v[64:79]\n"
      "ds_read_b64_tr_b16 v[220:221], v4 offset:44032\n"
      "ds_read_b64_tr_b16 v[222:223], v4 offset:44288\n"
      "global_load_dwordx4 v[120:123], v5, s[94:95] offset:1280 sc1\n"
      "v_rcp_f32 v21, v21\n"
      "v_fmac_f32 v28, v20, v22\n"
      "v_fmac_f32 v29, v21, v23\n"
      "v_mul_f32 v24, v54, v54\n"
      "s_waitcnt lgkmcnt(8)\n"
      "v_mfma_f32_32x32x16_bf16 v[64:79], v[228:231], v[164:167], v[64:79]\n"
      "ds_read_b64_tr_b16 v[224:225], v4 offset:45056\n"
      "ds_read_b64_tr_b16 v[226:227], v4 offset:45312\n"
      "global_load_dwordx4 v[124:127], v5, s[92:93] offset:0 sc1\n"
      "v_mul_f32 v25, v55, v55\n"
      "v_fma_f32 v24, v24, v240, v241\n"
      "v_fma_f32 v25, v25, v240, v241\n"
      "v_mul_f32 v24, v24, v54\n"
      "v_mul_f32 v25, v25, v55\n"
      "s_waitcnt lgkmcnt(8)\n"
      "v_mfma_f32_32x32x16_bf16 v[64:79], v[232:235], v[168:171], v[64:79]\n"
      "ds_read_b64_tr_b16 v[228:229], v4 offset:46080\n"
      "ds_read_b64_tr_b16 v[230:231], v4 offset:46336\n"
      "global_load_dwordx4 v[128:131], v5, s[92:93] offset:128 sc1\n"
      "v_exp_f32 v24, v24\n"
      "v_exp_f32 v25, v25\n"
      "v_mul_f32 v26, v54, v86\n"
      "s_waitcnt lgkmcnt(8)\n"
      "v_mfma_f32_32x32x16_bf16 v[64:79], v[236:239], v[172:175], v[64:79]\n"
      "ds_read_b64_tr_b16 v[232:233], v4 offset:47104\n"
      "ds_read_b64_tr_b16 v[234:235], v4 offset:47360\n"
      "global_load_dwordx4 v[132:135], v5, s[92:93] offset:256 sc1\n"
      "v_mul_f32 v27, v55, v87\n"
      "v_add_f32 v24, 1.0, v24\n"
      "v_add_f32 v25, 1.0, v25\n"
      "v_rcp_f32 v24, v24\n"
      "s_waitcnt lgkmcnt(8)\n"
      "v_mfma_f32_32x32x16_bf16 v[64:79], v[216:219], v[176:179], v[64:79]\n"
      "ds_read_b64_tr_b16 v[236:237], v4 offset:48128\n"
      "ds_read_b64_tr_b16 v[238:239], v4 offset:48384\n"
      "global_load_dwordx4 v[136:139], v5, s[92:93] offset:384 sc1\n"
      "v_rcp_f32 v25, v25\n"
      "v_fmac_f32 v28, v24, v26\n"
      "v_fmac_f32 v29, v25, v27\n"
      "v_mul_f32 v12, v56, v56\n"
      "s_waitcnt lgkmcnt(8)\n"
      "v_mfma_f32_32x32x16_bf16 v[64:79], v[220:223], v[180:183], v[64:79]\n"
      "ds_read_b64_tr_b16 v[216:217], v4 offset:49152\n"
      "ds_read_b64_tr_b16 v[218:219], v4 offset:49408\n"
      "global_load_dwordx4 v[140:143], v5, s[92:93] offset:1408 sc1\n"
      "v_mul_f32 v13, v57, v57\n"
      "v_fma_f32 v12, v12, v240, v241\n"
      "v_fma_f32 v13, v13, v240, v241\n"
      "v_mul_f32 v12, v12, v56\n"
      "v_mul_f32 v13, v13, v57\n"
      "s_waitcnt lgkmcnt(8)\n"
      "v_mfma_f32_32x32x16_bf16 v[64:79], v[224:227], v[184:187], v[64:79]\n"
      "ds_read_b64_tr_b16 v[220:221], v4 offset:50176\n"
      "ds_read_b64_tr_b16 v[222:223], v4 offset:50432\n"
      "global_load_dwordx4 v[144:147], v245, s[92:93] offset:0 sc1\n"
      "v_exp_f32 v12, v12\n"
      "v_exp_f32 v13, v13\n"
      "v_mul_f32 v14, v56, v88\n"
      "s_waitcnt lgkmcnt(8)\n"
      "v_mfma_f32_32x32x16_bf16 v[64:79], v[228:231], v[188:191], v[64:79]\n"
      "ds_read_b64_tr_b16 v[224:225], v4 offset:51200\n"
      "ds_read_b64_tr_b16 v[226:227], v4 offset:51456\n"
      "global_load_dwordx4 v[148:151], v5, s[92:93] offset:512 sc1\n"
      "v_mul_f32 v15, v57, v89\n"
      "v_add_f32 v12, 1.0, v12\n"
      "v_add_f32 v13, 1.0, v13\n"
      "v_rcp_f32 v12, v12\n"
      "s_waitcnt lgkmcnt(8)\n"
      "v_mfma_f32_32x32x16_bf16 v[64:79], v[232:235], v[192:195], v[64:79]\n"
      "ds_read_b64_tr_b16 v[228:229], v4 offset:52224\n"
      "ds_read_b64_tr_b16 v[230:231], v4 offset:52480\n"
      "global_load_dwordx4 v[152:155], v5, s[92:93] offset:640 sc1\n"
      "v_rcp_f32 v13, v13\n"
      "v_fmac_f32 v28, v12, v14\n"
      "v_fmac_f32 v29, v13, v15\n"
      "v_mul_f32 v16, v58, v58\n"
      "s_waitcnt lgkmcnt(8)\n"
      "v_mfma_f32_32x32x16_bf16 v[64:79], v[236:239], v[196:199], v[64:79]\n"
      "global_load_dwordx4 v[156:159], v5, s[92:93] offset:768 sc1\n"
      "v_mul_f32 v17, v59, v59\n"
      "v_fma_f32 v16, v16, v240, v241\n"
      "v_fma_f32 v17, v17, v240, v241\n"
      "v_mul_f32 v16, v16, v58\n"
      "v_mul_f32 v17, v17, v59\n"
      "s_waitcnt lgkmcnt(6)\n"
      "v_mfma_f32_32x32x16_bf16 v[64:79], v[216:219], v[200:203], v[64:79]\n"
      "global_load_dwordx4 v[160:163], v5, s[92:93] offset:896 sc1\n"
      "v_exp_f32 v16, v16\n"
      "v_exp_f32 v17, v17\n"
      "v_mul_f32 v18, v58, v90\n"
      "s_waitcnt lgkmcnt(4)\n"
      "v_mfma_f32_32x32x16_bf16 v[64:79], v[220:223], v[204:207], v[64:79]\n"
      "global_load_dwordx4 v[164:167], v5, s[92:93] offset:1024 sc1\n"
      "v_mul_f32 v19, v59, v91\n"
      "v_add_f32 v16, 1.0, v16\n"
      "v_add_f32 v17, 1.0, v17\n"
      "v_rcp_f32 v16, v16\n"
      "s_waitcnt lgkmcnt(2)\n"
      "v_mfma_f32_32x32x16_bf16 v[64:79], v[224:227], v[208:211], v[64:79]\n"
      "global_load_dwordx4 v[168:171], v5, s[92:93] offset:1152 sc1\n"
      "v_rcp_f32 v17, v17\n"
      "v_fmac_f32 v28, v16, v18\n"
      "v_fmac_f32 v29, v17, v19\n"
      "v_mul_f32 v20, v60, v60\n"
      "s_waitcnt lgkmcnt(0)\n"
      "v_mfma_f32_32x32x16_bf16 v[64:79], v[228:231], v[212:215], v[64:79]\n"
      "global_load_dwordx4 v[172:175], v5, s[92:93] offset:1280 sc1\n"
      "v_mul_f32 v21, v61, v61\n"
      "v_fma_f32 v20, v20, v240, v241\n"
      "v_fma_f32 v21, v21, v240, v241\n"
      "v_mul_f32 v20, v20, v60\n"
      "v_mul_f32 v21, v21, v61\n"
      "global_load_dwordx2 v[32:33], v6, s[54:55] sc1\n"
      "v_exp_f32 v20, v20\n"
      "v_exp_f32 v21, v21\n"
      "v_mul_f32 v22, v60, v92\n"
      "v_mul_f32 v23, v61, v93\n"
      "v_add_f32 v20, 1.0, v20\n"
      "v_add_f32 v21, 1.0, v21\n"
      "v_rcp_f32 v20, v20\n"
      "v_rcp_f32 v21, v21\n"
      "v_fmac_f32 v28, v20, v22\n"
      "v_fmac_f32 v29, v21, v23\n"
      "v_pk_mul_f32 v[24:25], v[62:63], v[62:63]\n"
      "v_pk_mul_f32 v[12:13], v[64:65], v[64:65]\n"
      "v_pk_fma_f32 v[24:25], v[24:25], v[240:241], v[240:241] op_sel:[0,0,1] op_sel_hi:[1,0,1]\n"
      "v_pk_fma_f32 v[12:13], v[12:13], v[240:241], v[240:241] op_sel:[0,0,1] op_sel_hi:[1,0,1]\n"
      "v_pk_mul_f32 v[24:25], v[24:25], v[62:63]\n"
      "v_pk_mul_f32 v[12:13], v[12:13], v[64:65]\n"
      "v_exp_f32 v24, v24\n"
      "v_exp_f32 v12, v12\n"
      "v_exp_f32 v25, v25\n"
      "v_exp_f32 v13, v13\n"
      "v_pk_mul_f32 v[26:27], v[62:63], v[94:95]\n"
      "v_pk_mul_f32 v[14:15], v[64:65], v[96:97]\n"
      "v_pk_add_f32 v[24:25], v[24:25], 1.0 op_sel_hi:[1,0]\n"
      "v_pk_add_f32 v[12:13], v[12:13], 1.0 op_sel_hi:[1,0]\n"
      "v_rcp_f32 v24, v24\n"
      "v_rcp_f32 v12, v12\n"
      "v_rcp_f32 v25, v25\n"
      "v_rcp_f32 v13, v13\n"
      "v_pk_fma_f32 v[28:29], v[24:25], v[26:27], v[28:29]\n"
      "v_pk_mul_f32 v[30:31], v[12:13], v[14:15]\n"
      "v_pk_mul_f32 v[16:17], v[66:67], v[66:67]\n"
      "v_pk_mul_f32 v[20:21], v[68:69], v[68:69]\n"
      "v_pk_fma_f32 v[16:17], v[16:17], v[240:241], v[240:241] op_sel:[0,0,1] op_sel_hi:[1,0,1]\n"
      "v_pk_fma_f32 v[20:21], v[20:21], v[240:241], v[240:241] op_sel:[0,0,1] op_sel_hi:[1,0,1]\n"
      "v_pk_mul_f32 v[16:17], v[16:17], v[66:67]\n"
      "v_pk_mul_f32 v[20:21], v[20:21], v[68:69]\n"
      "v_exp_f32 v16, v16\n"
      "v_exp_f32 v20, v20\n"
      "v_exp_f32 v17, v17\n"
      "v_exp_f32 v21, v21\n"
      "v_pk_mul_f32 v[18:19], v[66:67], v[98:99]\n"
      "v_pk_mul_f32 v[22:23], v[68:69], v[100:101]\n"
      "v_pk_add_f32 v[16:17], v[16:17], 1.0 op_sel_hi:[1,0]\n"
      "v_pk_add_f32 v[20:21], v[20:21], 1.0 op_sel_hi:[1,0]\n"
      "v_rcp_f32 v16, v16\n"
      "v_rcp_f32 v20, v20\n"
      "v_rcp_f32 v17, v17\n"
      "v_rcp_f32 v21, v21\n"
      "v_pk_fma_f32 v[30:31], v[16:17], v[18:19], v[30:31]\n"
      "v_pk_fma_f32 v[30:31], v[20:21], v[22:23], v[30:31]\n"
      "v_pk_mul_f32 v[24:25], v[70:71], v[70:71]\n"
      "v_pk_mul_f32 v[12:13], v[72:73], v[72:73]\n"
      "v_pk_fma_f32 v[24:25], v[24:25], v[240:241], v[240:241] op_sel:[0,0,1] op_sel_hi:[1,0,1]\n"
      "v_pk_fma_f32 v[12:13], v[12:13], v[240:241], v[240:241] op_sel:[0,0,1] op_sel_hi:[1,0,1]\n"
      "v_pk_mul_f32 v[24:25], v[24:25], v[70:71]\n"
      "v_pk_mul_f32 v[12:13], v[12:13], v[72:73]\n"
      "v_exp_f32 v24, v24\n"
      "v_exp_f32 v12, v12\n"
      "v_exp_f32 v25, v25\n"
      "v_exp_f32 v13, v13\n"
      "v_pk_mul_f32 v[26:27], v[70:71], v[102:103]\n"
      "v_pk_mul_f32 v[14:15], v[72:73], v[104:105]\n"
      "v_pk_add_f32 v[24:25], v[24:25], 1.0 op_sel_hi:[1,0]\n"
      "v_pk_add_f32 v[12:13], v[12:13], 1.0 op_sel_hi:[1,0]\n"
      "v_rcp_f32 v24, v24\n"
      "v_rcp_f32 v12, v12\n"
      "v_rcp_f32 v25, v25\n"
      "v_rcp_f32 v13, v13\n"
      "v_pk_fma_f32 v[30:31], v[24:25], v[26:27], v[30:31]\n"
      "v_pk_fma_f32 v[30:31], v[12:13], v[14:15], v[30:31]\n"
      "v_pk_mul_f32 v[16:17], v[74:75], v[74:75]\n"
      "v_pk_mul_f32 v[20:21], v[76:77], v[76:77]\n"
      "v_pk_fma_f32 v[16:17], v[16:17], v[240:241], v[240:241] op_sel:[0,0,1] op_sel_hi:[1,0,1]\n"
      "v_pk_fma_f32 v[20:21], v[20:21], v[240:241], v[240:241] op_sel:[0,0,1] op_sel_hi:[1,0,1]\n"
      "v_pk_mul_f32 v[16:17], v[16:17], v[74:75]\n"
      "v_pk_mul_f32 v[20:21], v[20:21], v[76:77]\n"
      "v_exp_f32 v16, v16\n"
      "v_exp_f32 v20, v20\n"
      "v_exp_f32 v17, v17\n"
      "v_exp_f32 v21, v21\n"
      "v_pk_mul_f32 v[18:19], v[74:75], v[106:107]\n"
      "v_pk_mul_f32 v[22:23], v[76:77], v[108:109]\n"
      "v_pk_add_f32 v[16:17], v[16:17], 1.0 op_sel_hi:[1,0]\n"
      "v_pk_add_f32 v[20:21], v[20:21], 1.0 op_sel_hi:[1,0]\n"
      "v_rcp_f32 v16, v16\n"
      "v_rcp_f32 v20, v20\n"
      "v_rcp_f32 v17, v17\n"
      "v_rcp_f32 v21, v21\n"
      "v_pk_fma_f32 v[30:31], v[16:17], v[18:19], v[30:31]\n"
      "v_pk_fma_f32 v[30:31], v[20:21], v[22:23], v[30:31]\n"
      "v_pk_mul_f32 v[24:25], v[78:79], v[78:79]\n"
      "v_pk_fma_f32 v[24:25], v[24:25], v[240:241], v[240:241] op_sel:[0,0,1] op_sel_hi:[1,0,1]\n"
      "v_pk_mul_f32 v[24:25], v[24:25], v[78:79]\n"
      "v_exp_f32 v24, v24\n"
      "v_exp_f32 v25, v25\n"
      "v_pk_mul_f32 v[26:27], v[78:79], v[110:111]\n"
      "v_pk_add_f32 v[24:25], v[24:25], 1.0 op_sel_hi:[1,0]\n"
      "v_rcp_f32 v24, v24\n"
      "v_rcp_f32 v25, v25\n"
      "s_nop 0\n"
      "v_pk_fma_f32 v[30:31], v[24:25], v[26:27], v[30:31]\n"
      "p1_sum:\n"
      "s_mov_b64 exec, 1\n"
      "ds_add_u32 v40, v41 offset:132\n"
      "s_mov_b64 exec, -1\n"
      "v_add_f32 v28, v28, v29\n"
      "v_add_f32 v30, v30, v31\n"
      "v_mov_b32 v36, s90\n"
      "v_and_b32 v30, s87, v30\n"
      "v_mov_b32 v38, s91\n"
      "v_add_f32 v28, v28, v30\n"
      "v_mov_b32 v29, v28\n"
      "v_cndmask_b32 v36, v36, v38, s[70:71]\n"
      "s_nop 0\n"
      "v_permlane32_swap_b32 v28, v29\n"
      "v_add_f32 v37, v28, v29\n"
      "s_branch p1_late\n"
      "p1_slow:\n"
      "s_mov_b32 s88, 0x400000\n"
      "9:\n"
      "ds_read_b32 v42, v40 offset:140\n"
      "s_sub_u32 s88, s88, 1\n"
      "s_waitcnt lgkmcnt(0)\n"
      "s_cbranch_scc1 p1_back\n"
      "v_cmp_gt_u32 vcc, 4, v42\n"
      "s_cbranch_vccnz 9b\n"
      "s_branch p1_back\n"
      "p1_nodnn:\n"
      "s_setprio 0\n"
      "v_mov_b32 v40, 159744\n"
      "v_mov_b32 v41, 1\n"
      "s_waitcnt vmcnt(0)\n"
      "v_readfirstlane_b32 s90, v44\n"
      "v_readfirstlane_b32 s91, v45\n"
      "s_nop 0\n"
      "s_add_u32 s90, s90, 0x9e3779b1\n"
      "s_add_u32 s91, s91, 0x85ebca77\n"
      "global_load_dwordx4 v[112:115], v5, s[94:95] offset:1024 sc1\n"
      "global_load_dwordx4 v[116:119], v5, s[94:95] offset:1152 sc1\n"
      "global_load_dwordx4 v[120:123], v5, s[94:95] offset:1280 sc1\n"
      "global_load_dwordx4 v[124:127], v5, s[92:93] offset:0 sc1\n"
      "global_load_dwordx4 v[128:131], v5, s[92:93] offset:128 sc1\n"
      "global_load_dwordx4 v[132:135], v5, s[92:93] offset:256 sc1\n"
      "global_load_dwordx4 v[136:139], v5, s[92:93] offset:384 sc1\n"
      "global_load_dwordx4 v[140:143], v5, s[92:93] offset:1408 sc1\n"
      "global_load_dwordx4 v[144:147], v245, s[92:93] offset:0 sc1\n"
      "global_load_dwordx4 v[148:151], v5, s[92:93] offset:512 sc1\n"
      "global_load_dwordx4 v[152:155], v5, s[92:93] offset:640 sc1\n"
      "global_load_dwordx4 v[156:159], v5, s[92:93] offset:768 sc1\n"
      "global_load_dwordx4 v[160:163], v5, s[92:93] offset:896 sc1\n"
      "global_load_dwordx4 v[164:167], v5, s[92:93] offset:1024 sc1\n"
      "global_load_dwordx4 v[168:171], v5, s[92:93] offset:1152 sc1\n"
      "global_load_dwordx4 v[172:175], v5, s[92:93] offset:1280 sc1\n"
      "global_load_dwordx2 v[32:33], v6, s[54:55] sc1\n"
      "p1_late:\n"
      "v_add_u32 v42, 8192, v11\n"
      "s_mov_b32 s88, 0x40000\n"
      "p1_le_a:\n"
      "s_waitcnt vmcnt(13)\n"
      "v_bitop3_b32 v13, v124, v126, s90 bitop3:0x96\n"
      "v_bitop3_b32 v14, v125, v127, s91 bitop3:0x96\n"
      "ds_write_b64 v7, v[124:125] offset:2592\n"
      "v_or_b32 v12, v13, v14\n"
      "s_waitcnt vmcnt(12)\n"
      "v_bitop3_b32 v13, v128, v130, s90 bitop3:0x96\n"
      "v_bitop3_b32 v14, v129, v131, s91 bitop3:0x96\n"
      "ds_write_b64 v7, v[128:129] offset:6048\n"
      "v_or3_b32 v12, v12, v13, v14\n"
      "s_waitcnt vmcnt(11)\n"
      "v_bitop3_b32 v13, v132, v134, s90 bitop3:0x96\n"
      "v_bitop3_b32 v14, v133, v135, s91 bitop3:0x96\n"
      "ds_write_b64 v7, v[132:133] offset:9504\n"
      "v_or3_b32 v12, v12, v13, v14\n"
      "s_waitcnt vmcnt(10)\n"
      "v_bitop3_b32 v13, v136, v138, s90 bitop3:0x96\n"
      "v_bitop3_b32 v14, v137, v139, s91 bitop3:0x96\n"
      "ds_write_b64 v7, v[136:137] offset:12960\n"
      "v_or3_b32 v12, v12, v13, v14\n"
      "s_waitcnt vmcnt(9)\n"
      "v_bitop3_b32 v13, v140, v142, s90 bitop3:0x96\n"
      "v_bitop3_b32 v14, v141, v143, s91 bitop3:0x96\n"
      "ds_write_b64 v8, v[140:141] offset:0\n"
      "v_or3_b32 v12, v12, v13, v14\n"
      "s_waitcnt vmcnt(8)\n"
      "v_bitop3_b32 v13, v144, v146, s90 bitop3:0x96\n"
      "v_bitop3_b32 v14, v145, v147, s91 bitop3:0x96\n"
      "ds_write_b64 v246, v[144:145] offset:0\n"
      "v_or3_b32 v12, v12, v13, v14\n"
      "v_cmp_ne_u32 vcc, 0, v12\n"
      "s_cbranch_vccz p1_le_b\n"
      "s_sleep 1\n"
      "s_waitcnt lgkmcnt(0)\n"
      "global_load_dwordx4 v[124:127], v5, s[92:93] offset:0 sc1\n"
      "global_load_dwordx4 v[128:131], v5, s[92:93] offset:128 sc1\n"
      "global_load_dwordx4 v[132:135], v5, s[92:93] offset:256 sc1\n"
      "global_load_dwordx4 v[136:139], v5, s[92:93] offset:384 sc1\n"
      "global_load_dwordx4 v[140:143], v5, s[92:93] offset:1408 sc1\n"
      "global_load_dwordx4 v[144:147], v245, s[92:93] offset:0 sc1\n"
      "s_waitcnt vmcnt(0)\n"
      "s_sub_u32 s88, s88, 1\n"
      "s_cbranch_scc0 p1_le_a\n"
      "p1_le_b:\n"
      "s_mov_b64 exec, 1\n"
      "ds_add_u32 v40, v41 offset:140\n"
      "s_mov_b64 exec, -1\n"
      "s_mov_b32 s88, 0x40000\n"
      "p1_ly_a:\n"
      "s_waitcnt vmcnt(10)\n"
      "v_bitop3_b32 v13, v112, v114, s90 bitop3:0x96\n"
      "v_bitop3_b32 v14, v113, v115, s91 bitop3:0x96\n"
      "ds_write_b64 v8, v[112:113] offset:29376\n"
      "v_or_b32 v12, v13, v14\n"
      "s_waitcnt vmcnt(9)\n"
      "v_bitop3_b32 v13, v116, v118, s90 bitop3:0x96\n"
      "v_bitop3_b32 v14, v117, v119, s91 bitop3:0x96\n"
      "ds_write_b64 v8, v[116:117] offset:31104\n"
      "v_or3_b32 v12, v12, v13, v14\n"
      "s_waitcnt vmcnt(8)\n"
      "v_bitop3_b32 v13, v120, v122, s90 bitop3:0x96\n"
      "v_bitop3_b32 v14, v121, v123, s91 bitop3:0x96\n"
      "ds_write_b64 v8, v[120:121] offset:32832\n"
      "v_or3_b32 v12, v12, v13, v14\n"
      "v_cmp_ne_u32 vcc, 0, v12\n"
      "s_cbranch_vccz p1_ly_b\n"
      "s_sleep 8\n"
      "s_waitcnt lgkmcnt(0)\n"
      "global_load_dwordx4 v[112:115], v5, s[94:95] offset:1024 sc1\n"
      "global_load_dwordx4 v[116:119], v5, s[94:95] offset:1152 sc1\n"
      "global_load_dwordx4 v[120:123], v5, s[94:95] offset:1280 sc1\n"
      "s_waitcnt vmcnt(0)\n"
      "s_sub_u32 s88, s88, 1\n"
      "s_cbranch_scc0 p1_ly_a\n"
      "p1_ly_b:\n"
      "s_mov_b32 s88, 0x40000\n"
      "p1_lg_a:\n"
      "s_waitcnt vmcnt(7)\n"
      "v_bitop3_b32 v13, v148, v150, s90 bitop3:0x96\n"
      "v_bitop3_b32 v14, v149, v151, s91 bitop3:0x96\n"
      "ds_write_b64 v7, v[148:149] offset:16416\n"
      "v_or_b32 v12, v13, v14\n"
      "s_waitcnt vmcnt(6)\n"
      "v_bitop3_b32 v13, v152, v154, s90 bitop3:0x96\n"
      "v_bitop3_b32 v14, v153, v155, s91 bitop3:0x96\n"
      "ds_write_b64 v7, v[152:153] offset:19872\n"
      "v_or3_b32 v12, v12, v13, v14\n"
      "s_waitcnt vmcnt(5)\n"
      "v_bitop3_b32 v13, v156, v158, s90 bitop3:0x96\n"
      "v_bitop3_b32 v14, v157, v159, s91 bitop3:0x96\n"
      "ds_write_b64 v7, v[156:157] offset:23328\n"
      "v_or3_b32 v12, v12, v13, v14\n"
      "s_waitcnt vmcnt(4)\n"
      "v_bitop3_b32 v13, v160, v162, s90 bitop3:0x96\n"
      "v_bitop3_b32 v14, v161, v163, s91 bitop3:0x96\n"
      "ds_write_b64 v7, v[160:161] offset:26784\n"
      "v_or3_b32 v12, v12, v13, v14\n"
      "s_waitcnt vmcnt(3)\n"
      "v_bitop3_b32 v13, v164, v166, s90 bitop3:0x96\n"
      "v_bitop3_b32 v14, v165, v167, s91 bitop3:0x96\n"
      "ds_write_b64 v8, v[164:165] offset:34560\n"
      "v_or3_b32 v12, v12, v13, v14\n"
      "s_waitcnt vmcnt(2)\n"
      "v_bitop3_b32 v13, v168, v170, s90 bitop3:0x96\n"
      "v_bitop3_b32 v14, v169, v171, s91 bitop3:0x96\n"
      "ds_write_b64 v8, v[168:169] offset:36288\n"
      "v_or3_b32 v12, v12, v13, v14\n"
      "s_waitcnt vmcnt(1)\n"
      "v_bitop3_b32 v13, v172, v174, s90 bitop3:0x96\n"
      "v_bitop3_b32 v14, v173, v175, s91 bitop3:0x96\n"
      "ds_write_b64 v8, v[172:173] offset:38016\n"
      "v_or3_b32 v12, v12, v13, v14\n"
      "v_cmp_ne_u32 vcc, 0, v12\n"
      "s_cbranch_vccz p1_lg_b\n"
      "s_sleep 8\n"
      "s_waitcnt lgkmcnt(0)\n"
      "global_load_dwordx4 v[148:151], v5, s[92:93] offset:512 sc1\n"
      "global_load_dwordx4 v[152:155], v5, s[92:93] offset:640 sc1\n"
      "global_load_dwordx4 v[156:159], v5, s[92:93] offset:768 sc1\n"
      "global_load_dwordx4 v[160:163], v5, s[92:93] offset:896 sc1\n"
      "global_load_dwordx4 v[164:167], v5, s[92:93] offset:1024 sc1\n"
      "global_load_dwordx4 v[168:171], v5, s[92:93] offset:1152 sc1\n"
      "global_load_dwordx4 v[172:175], v5, s[92:93] offset:1280 sc1\n"
      "s_waitcnt vmcnt(0)\n"
      "s_sub_u32 s88, s88, 1\n"
      "s_cbranch_scc0 p1_lg_a\n"
      "p1_lg_b:\n"
      "s_mov_b64 exec, 1\n"
      "ds_add_u32 v40, v41 offset:136\n"
      "s_mov_b64 exec, -1\n"
      "s_mov_b32 s88, 0x40000\n"
      "p1_fa:\n"
      "s_waitcnt vmcnt(0)\n"
      "v_cmp_ne_u64 vcc, s[90:91], v[32:33]\n"
      "s_cbranch_vccz p1_fb\n"
      "s_sleep 4\n"
      "global_load_dwordx2 v[32:33], v6, s[54:55] sc1\n"
      "s_sub_u32 s88, s88, 1\n"
      "s_cbranch_scc0 p1_fa\n"
      "p1_fb:\n"
      "global_load_dwordx4 v[176:179], v11, s[54:55] offset:-4096 sc1\n"
      "global_load_dwordx4 v[180:183], v11, s[54:55] offset:-3072 sc1\n"
      "global_load_dwordx4 v[184:187], v11, s[54:55] offset:-2048 sc1\n"
      "global_load_dwordx4 v[188:191], v11, s[54:55] offset:-1024 sc1\n"
      "global_load_dwordx4 v[192:195], v11, s[54:55] offset:0 sc1\n"
      "global_load_dwordx4 v[196:199], v11, s[54:55] offset:1024 sc1\n"
      "global_load_dwordx4 v[200:203], v11, s[54:55] offset:2048 sc1\n"
      "global_load_dwordx4 v[204:207], v11, s[54:55] offset:3072 sc1\n"
      "global_load_dwordx4 v[208:211], v42, s[54:55] offset:-4096 sc1\n"
      "global_load_dwordx4 v[212:215], v42, s[54:55] offset:-3072 sc1\n"
      "s_setprio 3\n"
      "s_cmp_lt_i32 s59, 0\n"
      "s_cbranch_scc1 p1_end\n"
      "global_store_dwordx2 v10, v[36:37], s[54:55] sc1\n"
      "s_waitcnt vmcnt(1)\n"
      "s_branch p1_end2\n"
      "p1_end:\n"
      "s_waitcnt vmcnt(0)\n"
      "p1_end2:\n"
      "s_waitcnt lgkmcnt(0)\n"
      "x_done:\n"
      "s_nop 1\n"
      : "=&{v[176:179]}"(buf[0]), "=&{v[180:183]}"(buf[1]), "=&{v[184:187]}"(buf[2]), "=&{v[188:191]}"(buf[3]), "=&{v[192:195]}"(buf[4]), "=&{v[196:199]}"(buf[5]), "=&{v[200:203]}"(buf[6]), "=&{v[204:207]}"(buf[7]), "=&{v[208:211]}"(buf[8]), "=&{v[212:215]}"(buf[9]), "=&{s[90:91]}"(tg)
      : "{s[36:51]}"(ka0), "{s[52:55]}"(ka1), "{s56}"(wave), "{s57}"(x), "{s58}"(y), [tid] "{v0}"(tid), [bid] "s"(bid), [kp] "s"(__builtin_amdgcn_kernarg_segment_ptr())
      : "memory", "scc", "v1", "v2", "v3", "v4", "v5", "v6", "v7", "v8", "v9", "v10", "v11", "v12", "v13", "v14", "v15", "v16", "v17", "v18", "v19", "v20", "v21", "v22", "v23", "v24", "v25", "v26", "v27", "v28", "v29", "v30", "v31", "v32", "v33", "v34", "v35", "v36", "v37", "v38", "v39", "v40", "v41", "v42", "v43", "v44", "v45", "v46", "v47", "v48", "v49", "v50", "v51", "v52", "v53", "v54", "v55", "v56", "v57", "v58", "v59", "v60", "v61", "v62", "v63", "v64", "v65", "v66", "v67", "v68", "v69", "v70", "v71", "v72", "v73", "v74", "v75", "v76", "v77", "v78", "v79", "v80", "v81", "v82", "v83", "v84", "v85", "v86", "v87", "v88", "v89", "v90", "v91", "v92", "v93", "v94", "v95", "v96", "v97", "v98", "v99", "v100", "v101", "v102", "v103", "v104", "v105", "v106", "v107", "v108", "v109", "v110", "v111", "v112", "v113", "v114", "v115", "v116", "v117", "v118", "v119", "v120", "v121", "v122", "v123", "v124", "v125", "v126", "v127", "v128", "v129", "v130", "v131", "v132", "v133", "v134", "v135", "v136", "v137", "v138", "v139", "v140", "v141", "v142", "v143", "v144", "v145", "v146", "v147", "v148", "v149", "v150", "v151", "v152", "v153", "v154", "v155", "v156", "v157", "v158", "v159", "v160", "v161", "v162", "v163", "v164", "v165", "v166", "v167", "v168", "v169", "v170", "v171", "v172", "v173", "v174", "v175", "v216", "v217", "v218", "v219", "v220", "v221", "v222", "v223", "v224", "v225", "v226", "v227", "v228", "v229", "v230", "v231", "v232", "v233", "v234", "v235", "v236", "v237", "v238", "v239", "v240", "v241", "v242", "v243", "v244", "v245", "v246", "v247", "v248", "v249", "v250", "v251", "v252", "v253", "v254", "v255", "s59", "s60", "s61", "s62", "s63", "s64", "s65", "s66", "s67", "s68", "s69", "s70", "s71", "s72", "s73", "s74", "s75", "s76", "s77", "s78", "s79", "s80", "s81", "s82", "s83", "s84", "s85", "s86", "s87", "s88", "s89", "s92", "s93", "s94", "s95", "vcc");
  asm volatile("" : "+{s[36:51]}"(ka0), "+{s[52:55]}"(ka1));
  const Args a = args();
  const u64 tag = (u64)(u32)__builtin_amdgcn_readfirstlane(tg[0]) | ((u64)(u32)__builtin_amdgcn_readfirstlane(tg[1]) << 32);
  if (wave < 4) {
    const int h = wave, ln = tid & 63;
    const __amdgpu_buffer_rsrc_t rs = __builtin_amdgcn_make_buffer_rsrc(a.ws, 0, WS_USED, 0x00020000);
    constexpr int cum[5] = {0, 0, 10, 19, 28};
    const u32 goff = WS_AF + 46 * FRAG + h * 1024 + ln * 16;
    LDS_AS char* ldst = l + 46 * FRAG + h * 1024 + ln * 16;
#define LD(i0, i1) _Pragma("unroll") for (int i = i0; i < i1; ++i) buf[i - i0] = __builtin_amdgcn_raw_buffer_load_b128(rs, goff + i * 4096, 0, 16)
#define WR(i0, j0, j1) _Pragma("unroll") for (int i = j0; i < j1; ++i) *(LDS_AS u32x4*)(ldst + i * 4096) = buf[i - i0]
#define BARRIER() asm volatile("s_waitcnt lgkmcnt(0)\n\ts_barrier" ::: "memory")
    __builtin_amdgcn_s_setprio(LOADER_PRIO);
    wait_lds((LDS_AS u32*)(l + L_MISC + 136), 4u);
#pragma unroll
    for (int i = 0; i < 10; ++i) *(LDS_AS u32x4*)(ldst + h * 9216 + i * 1024) = buf[i];
    if (h == 2) {
      const u32x4 c = __builtin_amdgcn_raw_buffer_load_b128(rs, WS_CST, 0, 16);
      const u32 d = __builtin_amdgcn_raw_buffer_load_b32(rs, WS_CST + 16, 0, 16);
      const u32 a0 = *(LDS_AS u32*)(l + L_MISC + 256 + 128);
      *(LDS_AS u32x4*)(l + L_MISC + 16) = u32x4{c[1] + a0 - 134, c[3] + c[0] + a0 - 268, d + c[2] + c[0] + a0 - 402, 0u};
    }
    LD(cum[2], cum[3]);
    BARRIER();
    if (h == 2 && ln == 0) __hip_atomic_store((GLB_AS u64*)(a.ws + WS_ARR + bid * 8), tag, RLX_AGENT);
    WR(cum[2], cum[2], cum[3]);
    LD(cum[3], cum[4]);
    BARRIER();
    WR(cum[3], cum[3], cum[4]);
    if (h == 0) {
      const int s = ln & 15, n0 = ln >> 4;
      const u32 o0 = WS_PART + (n0 * NB + bid * 16 + s) * 16, o1 = WS_PART + (min(n0 + 4, 6) * NB + bid * 16 + s) * 16;
      u32x4 g0, g1;
      _Pragma("nounroll") for (u32 spins = 0; spins < SPIN_CAP; ++spins) {
        g0 = __builtin_amdgcn_raw_buffer_load_b128(rs, o0, 0, 16); g1 = __builtin_amdgcn_raw_buffer_load_b128(rs, o1, 0, 16);
        const bool ok = g0[0] == (u32)tag && g0[2] == (u32)(tag >> 32) && g1[0] == (u32)tag && g1[2] == (u32)(tag >> 32) && g0[1] == g0[3] && g1[1] == g1[3];
        if (__all(ok)) break;
        __builtin_amdgcn_s_sleep(2);
      }
      float v = __uint_as_float(g0[1]) + (n0 < 3 ? __uint_as_float(g1[1]) : bf2f(*(const GLB_AS u16*)a.bout));
      v += __shfl_xor(v, 16); v += __shfl_xor(v, 32);
      *(LDS_AS float*)(l + L_MISC + 64 + s * 4) = v;
    }
    BARRIER();
    if (h == 1 && bid == 0) {
#pragma unroll
      for (int i = 0; i < 4; ++i) wait_words(a.ws + WS_ARR + i * 512, 8, 64, tag, ln);
      if (ln == 0) __hip_atomic_store((GLB_AS u64*)(a.ws + WS_EPOCH), tag, RLX_AGENT);
    }
    asm volatile("xdf_e0:");
  }
}

extern "C" void kernel_launch(void* const* d_in, const int* in_sizes, int n_in,
                              void* d_out, int out_size, void* d_ws, size_t ws_size,
                              hipStream_t stream) {
  Args a;
  a.idx = (const int*)d_in[0]; a.table = (const u16*)d_in[1]; a.W = (const u16*)d_in[2]; a.wout = (const u16*)d_in[3];
  a.w1 = (const u16*)d_in[4]; a.b1 = (const u16*)d_in[5]; a.w2 = (const u16*)d_in[6]; a.bout = (const u16*)d_in[7];
  a.out = (u16*)d_out; a.ws = (char*)d_ws;
  xdeepfm_kernel<<<NB / 16, 512, 0, stream>>>(a);
}
```
